```python
import torch

def _softmax(x, axis=-1):
    return torch.softmax(x, dim=axis)

def _gelu(x):
    return torch.nn.functional.gelu(x, approximate='tanh')

def _silu(x):
    return torch.nn.functional.silu(x)

def _sigmoid(x):
    return torch.sigmoid(x)

def _layernorm(x, axis=-1, eps=1e-05):
    m = x.mean(dim=axis, keepdim=True)
    v = x.var(dim=axis, keepdim=True, unbiased=False)
    return (x - m) / torch.sqrt(v + eps)

def _scatter_add(tgt, idx, src, axis=0):
    return tgt.clone().index_add_(axis, idx, src)

def _segment_sum(src, seg, n):
    out = torch.zeros((n,) + src.shape[1:], dtype=src.dtype)
    return out.index_add_(0, seg, src)

def _pad(x, pw):
    flat = tuple((v for pair in reversed(pw) for v in pair))
    return torch.nn.functional.pad(x, flat)
kernel_name = 'sequential_preLN__memory_aug_recurrent_mem_tr__mixer_gmlp__prompt'

def setup_inputs():

    _w = lambda a: (lambda t: t.to(torch.bfloat16) if t.is_floating_point() else t)(torch.as_tensor(a))

    def _A_setup():
        B, S, Dm = (1, 2048, 640)
        H, Dh = (32, 192)
        M, seg = (8, 32)
        seg = min(seg, S)
        S = S // seg * seg
        return {'x': _w((lambda *s: torch.randn(*s, dtype=torch.bfloat16) * 0.05)(B, S, Dm)), 'mem0': _w((lambda *s: torch.randn(*s, dtype=torch.bfloat16) * 0.2)(B, M, Dm)), 'Wq': _w((lambda *s: torch.randn(*s, dtype=torch.bfloat16) * 1.6)(Dm, H, Dh) * Dm ** (-0.5)), 'Wk': _w((lambda *s: torch.randn(*s, dtype=torch.bfloat16) * 1.6)(Dm, H, Dh) * Dm ** (-0.5)), 'Wv': _w((lambda *s: torch.randn(*s, dtype=torch.bfloat16) * 0.2)(Dm, H, Dh) * Dm ** (-0.5)), 'Wo': _w((lambda *s: torch.randn(*s, dtype=torch.bfloat16) * 0.2)(H, Dh, Dm) * (H * Dh) ** (-0.5)), 'seg_len': seg}
    _A_in = _A_setup()

    def _B_setup():
        B, S, D, F = (1, 2048, 640, 1536)
        Fh = F // 2
        return {'x': _w((lambda *s: torch.randn(*s, dtype=torch.bfloat16) * 0.05)(B, S, D)), 'w_in': _w((lambda *s: torch.randn(*s, dtype=torch.bfloat16) * 0.4)(D, 2 * Fh)), 'w_sp': _w(0.01 * (lambda *s: torch.randn(*s, dtype=torch.bfloat16) * 0.4)(S, S)), 'b_sp': _w(1.0 + 0.01 * (lambda *s: torch.randn(*s, dtype=torch.bfloat16) * 0.05)(S)), 'w_out': _w((lambda *s: torch.randn(*s, dtype=torch.bfloat16) * 0.05)(Fh, D))}
    _B_in = _B_setup()
    return {'A_x': _A_in['x'], 'A_mem0': _A_in['mem0'], 'A_Wq': _A_in['Wq'], 'A_Wk': _A_in['Wk'], 'A_Wv': _A_in['Wv'], 'A_Wo': _A_in['Wo'], 'A_seg_len': _A_in['seg_len'], 'B_x': _B_in['x'], 'B_w_in': _B_in['w_in'], 'B_w_sp': _B_in['w_sp'], 'B_b_sp': _B_in['b_sp'], 'B_w_out': _B_in['w_out']}

def reference(A_x, A_mem0, A_Wq, A_Wk, A_Wv, A_Wo, A_seg_len, B_x, B_w_in, B_w_sp, B_b_sp, B_w_out):

    def _A_ref(x, mem0, Wq, Wk, Wv, Wo, seg_len):
        B, S, Dm = x.shape
        H, Dh = (Wq.shape[1], Wq.shape[2])
        M = mem0.shape[1]
        scale = Dh ** (-0.5)
        n_seg = S // seg_len
        L = M + seg_len
        idx = torch.arange(L)
        mask = (idx[:, None] < M) | (idx[:, None] >= idx[None, :])
        mem = mem0
        outs = []
        for s in range(n_seg):
            xs = x[:, s * seg_len:(s + 1) * seg_len, :].clone()
            h = torch.concatenate([mem, xs], axis=1)
            q = torch.einsum('btd,dhe->bhte', h, Wq)
            k = torch.einsum('btd,dhe->bhte', h, Wk)
            v = torch.einsum('btd,dhe->bhte', h, Wv)
            scores = torch.einsum('bhqe,bhke->bhqk', q, k) * scale
            scores = torch.where(mask[None, None], scores, -1000000000.0)
            p = _softmax(scores, axis=-1)
            o = torch.einsum('bhqk,bhke->bhqe', p, v)
            o = torch.einsum('bhte,hed->btd', o, Wo)
            mem = o[:, :M, :].clone()
            outs.append(o[:, M:, :])
        return torch.concatenate(outs, axis=1)

    def _B_ref(x, w_in, w_sp, b_sp, w_out):
        B, S, D = x.shape
        F = w_in.shape[-1]
        Fh = F // 2
        z = _gelu(torch.einsum('bsd,df->bsf', x, w_in))
        z1 = z[..., :Fh].clone()
        z2 = _layernorm(z[..., Fh:], axis=-1)
        g = torch.einsum('ts,bsf->btf', w_sp, z2) + b_sp[None, :, None]
        return torch.einsum('bsf,fd->bsd', z1 * g, w_out)
    x = A_x
    h = x + _A_ref(_layernorm(x, axis=-1), A_mem0, A_Wq, A_Wk, A_Wv, A_Wo, A_seg_len)
    h2 = h + _B_ref(_layernorm(h, axis=-1), B_w_in, B_w_sp, B_b_sp, B_w_out)
    return h2
```

```cpp
#include <type_traits>
#include <hip/hip_runtime.h>
#include <hip/hip_bf16.h>
#include <stdint.h>

typedef __bf16 bf16x4 __attribute__((ext_vector_type(4)));
typedef __bf16 bf16x8 __attribute__((ext_vector_type(8)));
typedef __bf16 bf16x16 __attribute__((ext_vector_type(16)));
typedef __bf16 bf16x2 __attribute__((ext_vector_type(2)));
typedef float f32x2 __attribute__((ext_vector_type(2)));
typedef float f32x4 __attribute__((ext_vector_type(4)));
typedef unsigned u32x2 __attribute__((ext_vector_type(2)));
typedef unsigned u32x4 __attribute__((ext_vector_type(4)));
typedef unsigned u32x8 __attribute__((ext_vector_type(8)));
typedef short s16x4 __attribute__((ext_vector_type(4)));
#define GAS __attribute__((address_space(1)))
#define LAS __attribute__((address_space(3)))
#define DEV __device__ __forceinline__
typedef __amdgpu_buffer_rsrc_t rsrc_t;

constexpr int SEQ = 2048, DM = 640, NH = 32, DH = 192, NSEG = 64, FF = 1536, FH = 768;
constexpr unsigned SENT = 0xFFFFFFFFu;
constexpr unsigned OOB = 0x7FFFFFF0u;

DEV rsrc_t mk_rsrc(const void* p, unsigned bytes) { return __builtin_amdgcn_make_buffer_rsrc((void*)p, 0, bytes, 0x00020000); }
DEV u32x4 mk_srd(const void* p, unsigned bytes) {
  unsigned long long a = (unsigned long long)p;
  u32x4 r; r.x = __builtin_amdgcn_readfirstlane((unsigned)a); r.y = __builtin_amdgcn_readfirstlane((unsigned)(a >> 32) & 0xFFFFu);
  r.z = bytes; r.w = 0x00020000u; return r;
}
DEV u32x4 ld16_dev(rsrc_t r, unsigned voff, unsigned soff) { return __builtin_amdgcn_raw_buffer_load_b128(r, voff, soff, 16); }
DEV u32x4 ld16(rsrc_t r, unsigned voff, unsigned soff) { return __builtin_amdgcn_raw_buffer_load_b128(r, voff, soff, 0); }
DEV void st_guard(u32x4 v) { asm volatile("s_nop 1" :: "v"(v) : "memory"); }
DEV void st16_dev_ng(u32x4 v, rsrc_t r, unsigned voff, unsigned soff) { __builtin_amdgcn_raw_buffer_store_b128(v, r, voff, soff, 16); }
DEV void st16_dev(u32x4 v, rsrc_t r, unsigned voff, unsigned soff) { st16_dev_ng(v, r, voff, soff); st_guard(v); }
DEV void dma_base(unsigned ldsaddr) { asm volatile("s_mov_b32 m0, %0" :: "s"(ldsaddr) : "memory"); }
template <int OFF, bool DEVSCOPE> DEV void dma_at(u32x4 srd, unsigned voff, unsigned soff) {
  if (DEVSCOPE) asm volatile("buffer_load_dwordx4 %0, %1, %2 offen offset:%3 sc1 lds" :: "v"(voff), "s"(srd), "s"(soff), "n"(OFF) : "memory");
  else asm volatile("buffer_load_dwordx4 %0, %1, %2 offen offset:%3 lds" :: "v"(voff), "s"(srd), "s"(soff), "n"(OFF) : "memory");
}
DEV void dma16(u32x4 srd, unsigned voff, unsigned soff, unsigned ldsaddr) {
  asm volatile("s_mov_b32 m0, %3\n\ts_nop 0\n\tbuffer_load_dwordx4 %0, %1, %2 offen lds" :: "v"(voff), "s"(srd), "s"(soff), "s"(ldsaddr) : "memory");
}
DEV void dma16_dev(u32x4 srd, unsigned voff, unsigned soff, unsigned ldsaddr) {
  asm volatile("s_mov_b32 m0, %3\n\ts_nop 0\n\tbuffer_load_dwordx4 %0, %1, %2 offen sc1 lds" :: "v"(voff), "s"(srd), "s"(soff), "s"(ldsaddr) : "memory");
}
#define WAIT_VM(n) asm volatile("s_waitcnt vmcnt(" #n ")" ::: "memory")
#define WAIT_LGKM(n) asm volatile("s_waitcnt lgkmcnt(" #n ")" ::: "memory")
#define WAIT_LGKM0() asm volatile("s_waitcnt lgkmcnt(0)" ::: "memory")
DEV void bar() { asm volatile("s_waitcnt lgkmcnt(0)\n\ts_barrier" ::: "memory"); }
DEV u32x2 tr_read(LAS char* p) { return __builtin_bit_cast(u32x2, __builtin_amdgcn_ds_read_tr16_b64_v4i16((LAS s16x4*)p)); }
DEV u32x4 lds_r16(LAS char* p) { return *(LAS u32x4*)p; }
DEV void lds_w16(LAS char* p, u32x4 v) { *(LAS u32x4*)p = v; }
DEV unsigned pk_bf16(float lo, float hi) { f32x2 f = {lo, hi}; return __builtin_bit_cast(unsigned, __builtin_convertvector(f, bf16x2)); }
DEV unsigned max3u(unsigned a, unsigned b, unsigned c) { return max(max(a, b), c); }
DEV unsigned smax(unsigned m, u32x4 v) { return max3u(max3u(m, v.x, v.y), v.z, v.w); }
DEV bool any_sent(unsigned m) { return __builtin_amdgcn_ballot_w64(m == SENT) != 0; }
DEV unsigned san(unsigned v) { return min(v, 0xFFFFFFFEu); }
DEV bf16x16 cat(u32x4 a, u32x4 b) { u32x8 r = {a.x, a.y, a.z, a.w, b.x, b.y, b.z, b.w}; return __builtin_bit_cast(bf16x16, r); }
DEV f32x4 smf(u32x4 a, bf16x16 b, f32x4 c, int idx) { return __builtin_amdgcn_smfmac_f32_16x16x64_bf16(__builtin_bit_cast(bf16x8, a), b, c, idx, 0, 0); }
DEV f32x4 mma(u32x4 a, u32x4 b, f32x4 c) { return __builtin_amdgcn_mfma_f32_16x16x32_bf16(__builtin_bit_cast(bf16x8, a), __builtin_bit_cast(bf16x8, b), c, 0, 0, 0); }
DEV void swap16(unsigned& a, unsigned& b) { auto r = __builtin_amdgcn_permlane16_swap(a, b, false, false); a = r[0]; b = r[1]; }
DEV void swap32(unsigned& a, unsigned& b) { auto r = __builtin_amdgcn_permlane32_swap(a, b, false, false); a = r[0]; b = r[1]; }
DEV float fmx(float a, float b) { return __builtin_elementwise_maximum(a, b); }
DEV float fmx3(float a, float b, float c) { return fmx(fmx(a, b), c); }
DEV unsigned pk_sum(f32x4 d) { f32x2 lo = {d[0], d[1]}, hi = {d[2], d[3]}; return __builtin_bit_cast(unsigned, __builtin_convertvector(lo + hi, bf16x2)); }
DEV void mma_v0(f32x4& d, u32x4 a, u32x4 b) { asm volatile("v_mfma_f32_16x16x32_bf16 %0, %1, %2, 0" : "=&v"(d) : "v"(a), "v"(b)); }
DEV void mma_v(f32x4& d, u32x4 a, u32x4 b) { asm volatile("v_mfma_f32_16x16x32_bf16 %0, %1, %2, %0" : "+v"(d) : "v"(a), "v"(b)); }
DEV void mma_a0(f32x4& d, u32x4 a, u32x4 b) { asm volatile("v_mfma_f32_16x16x32_bf16 %0, %1, %2, 0" : "=&v"(d) : "v"(a), "a"(b)); }
DEV void mma_a(f32x4& d, u32x4 a, u32x4 b) { asm volatile("v_mfma_f32_16x16x32_bf16 %0, %1, %2, %0" : "+v"(d) : "v"(a), "a"(b)); }
DEV void mma_w0(f32x4& d, u32x4 a, u32x4 b) { asm volatile("v_mfma_f32_16x16x32_bf16 %0, %1, %2, 0" : "=&v"(d) : "a"(a), "v"(b)); }
DEV void mma_w(f32x4& d, u32x4 a, u32x4 b) { asm volatile("v_mfma_f32_16x16x32_bf16 %0, %1, %2, %0" : "+v"(d) : "a"(a), "v"(b)); }
DEV unsigned long long sent_mask(unsigned m) { return __builtin_amdgcn_ballot_w64(m == SENT); }
#define SB() __builtin_amdgcn_sched_barrier(0)
#define PINV(x) asm volatile("" : "+v"(x))
#define PINS(x) asm volatile("" : "+s"(x))
DEV unsigned rt32() { return (unsigned)__builtin_amdgcn_s_memrealtime(); }
DEV unsigned cyc32() { return (unsigned)__builtin_amdgcn_s_memtime(); }
DEV void clock_pair(unsigned& now, unsigned& c0) { const unsigned r0 = rt32(); int n = 0; do { now = rt32(); c0 = cyc32(); SB(); } while (now == r0 && ++n < 64); }
DEV void idle_from(unsigned c0, int r) {
  const unsigned tgt = c0 + (unsigned)(r > 32768 ? 32768 : r);
  int rem;
  do { rem = (int)(tgt - cyc32()); } while (rem > 110);
  asm volatile("s_sub_i32 %0, %0, 40\n\ts_cmp_gt_i32 %0, 0\n\ts_cbranch_scc0 2f\n1:\n\ts_sub_i32 %0, %0, 28\n\ts_cmp_gt_i32 %0, 0\n\ts_cbranch_scc1 1b\n2:" : "+s"(rem) :: "scc");
}
DEV int since(unsigned now, unsigned t) { return (((int)((now - t) << 8)) >> 8) * 24; }
DEV bool any_nan(float a, float b) { return __builtin_amdgcn_ballot_w64(__builtin_isunordered(a, b)) != 0; }
DEV unsigned long long nan_mask(float a, float b) { return __builtin_amdgcn_ballot_w64(__builtin_isunordered(a, b)); }
DEV float xg_max(float x) {
  unsigned a = __float_as_uint(x), b = a; swap16(a, b); x = fmx(__uint_as_float(a), __uint_as_float(b));
  a = __float_as_uint(x); b = a; swap32(a, b); return fmx(__uint_as_float(a), __uint_as_float(b));
}
DEV float xg_sum(float x) {
  unsigned a = __float_as_uint(x), b = a; swap16(a, b); x = __uint_as_float(a) + __uint_as_float(b);
  a = __float_as_uint(x); b = a; swap32(a, b); return __uint_as_float(a) + __uint_as_float(b);
}
template <int CTRL> DEV unsigned dpp(unsigned v) { return __builtin_amdgcn_update_dpp(0u, v, CTRL, 0xF, 0xF, true); }
#define BACK 300
#define NEXTD 3300
#define PRED -400
#define DSLEEP 0
#define DREAD 1800
#define BSLEEP 3
#define VGAP -1
#define PRE 700
#define EV(i)
#define PARFLIP 0
constexpr float SCALE_LOG2E = 0.07216878364870322f * 1.4426950408889634f;
#define ROT 0
#define RROT 1
#define GAP 8
#define STR_(x) #x
#define STR(x) STR_(x)
constexpr size_t W_RING = 0;
constexpr size_t W_FLAG = 2u << 20;
constexpr size_t W_STAT = W_FLAG + 163840;
constexpr size_t W_XN = W_STAT + 65536;
constexpr size_t W_KX = W_XN + (size_t)NSEG * 40960;
constexpr size_t W_V = W_KX + (size_t)NSEG * NH * 12288;
constexpr size_t W_VOX = W_V + (size_t)NSEG * NH * 12288;
constexpr size_t W_KM = W_VOX + (size_t)NSEG * 40 * NH * 1024;
constexpr size_t W_VOM = W_KM + (size_t)NSEG * NH * 3072;
constexpr size_t W_PX = W_VOM + (size_t)NSEG * 40 * 8192;
constexpr size_t W_PM = W_PX + (size_t)NSEG * 2 * NH * 1024;
constexpr size_t W_H = W_PM + (size_t)NSEG * 2 * 8 * 1024;
constexpr size_t W_Z1 = W_H + (size_t)NSEG * 40960;
constexpr size_t W_Z2 = W_Z1 + (size_t)NSEG * 49152;
constexpr size_t W_U = W_Z2 + (size_t)NSEG * 49152;
constexpr size_t W_DBG = W_U + (size_t)NSEG * 49152;
constexpr size_t W_END = W_DBG + (1u << 20);
constexpr int FL_CTL = 0, FL_PAR = 2, FL_KX = 16, FL_V = FL_KX + 128, FL_VOX = FL_V + 128, FL_KM = FL_VOX + 128, FL_VOM = FL_KM + 32,
              FL_PX = FL_VOM + 128, FL_H = FL_PX + 64, FL_Z = FL_H + 96, FL_U = FL_Z + 128, FL_RDY = FL_U + 256, FL_END = FL_RDY + 64;
static_assert(FL_END <= 1280, "flag area");

struct P {
  const void* x; const void* mem0; const void* Wq; const void* Wk; const void* Wv; const void* Wo;
  const void* w_in; const void* w_sp; const void* b_sp; const void* w_out;
  void* out; char* ws;
  int dq, dv, dr, dx;
};
constexpr unsigned RING_BYTES = 2u << 20;
constexpr unsigned CH = 16384;
DEV unsigned mem_off(int slot, int ks) { return (unsigned)(2 * slot + (ks >> 3)) * CH + (unsigned)(ks & 7) * 1024u; }
DEV unsigned px_off(int slot, int kb) { return (unsigned)(6 + 3 * slot + (kb >> 3)) * CH + (unsigned)(kb & 7) * 1024u; }
DEV unsigned pm_off(int slot) { return (unsigned)(8 + 3 * slot) * CH; }
DEV unsigned vo_off(int slot, int ct) { return (unsigned)(15 + 40 * slot + ct) * CH; }
constexpr int SPIN_MAX = 1 << 18;

DEV bool give_up(int& spins, char* ws) {
  unsigned* ctl = (unsigned*)(ws + W_FLAG);
  if (++spins > SPIN_MAX) { __hip_atomic_store((GAS unsigned*)ctl, 1u, __ATOMIC_RELAXED, __HIP_MEMORY_SCOPE_AGENT); return true; }
  if ((spins & 1023) == 0) return __builtin_amdgcn_readfirstlane(__hip_atomic_load((GAS unsigned*)ctl, __ATOMIC_RELAXED, __HIP_MEMORY_SCOPE_AGENT)) == 1u;
  return false;
}
DEV rsrc_t flag_rsrc(const P& p) { return mk_rsrc(p.ws + W_FLAG, 163840); }
DEV void flag_set(rsrc_t rf, int lane, int word, unsigned v) { __builtin_amdgcn_raw_buffer_store_b32(v, rf, lane == 0 ? 0u : OOB, (unsigned)word * 128u, 16); }
template <bool NAP = true>
DEV int flag_wait(const P& p, rsrc_t rf, int lane, int word0, int stride, int n, int need, bool& dead) {
  int spins = 0; int v;
  for (;;) {
    asm volatile("" ::: "memory");
    v = (int)__builtin_amdgcn_raw_buffer_load_b32(rf, (unsigned)((lane & (n - 1)) * stride) * 128u, (unsigned)word0 * 128u, 16);
    if (__builtin_amdgcn_ballot_w64(v < need) == 0) break;
    if (give_up(spins, p.ws)) { dead = true; return need; }
    if (NAP) __builtin_amdgcn_s_sleep(2);
  }
  int r = need;
  if (__builtin_amdgcn_ballot_w64(v < need + 1) == 0) r = need + 1;
  if (__builtin_amdgcn_ballot_w64(v < need + 4) == 0) r = need + 4;
  if (__builtin_amdgcn_ballot_w64(v < need + 8) == 0) r = need + 8;
  if (__builtin_amdgcn_ballot_w64(v < need + 16) == 0) r = need + 16;
  return r;
}
DEV char* ring_base(const P& p) {
  const unsigned v = ((const unsigned*)(p.ws + W_FLAG))[FL_PAR * 32 + (threadIdx.x & 63)];
  return p.ws + W_RING + (__builtin_popcountll(__builtin_amdgcn_ballot_w64(v != 0)) > 32 ? 8192 : 0);
}
DEV int tick_diff(unsigned a, unsigned b) { return ((int)((a - b) << 8)) >> 8; }
DEV unsigned stamp(unsigned t, int s) { return (t & 0xFFFFFFu) | ((unsigned)s << 24); }
DEV unsigned nx_off(int s) { return 95u * CH + (unsigned)s * 128u; }
DEV unsigned dl_off(int s) { return (unsigned)(96 + ((s >> 1) & 1)) * CH + (unsigned)(s & 1) * 4096u; }
DEV void report_late(rsrc_t rr, unsigned vdl, int s, unsigned t) { __builtin_amdgcn_raw_buffer_store_b32(stamp(t, s), rr, vdl, dl_off(s), 16); }
DEV unsigned next_start(const P& p, rsrc_t rr, int s, unsigned tn, bool& dead, unsigned& now, unsigned& c0) {
  if (s > 0) { now = rt32(); c0 = cyc32(); idle_from(c0, NEXTD - since(now, tn)); }
  unsigned v; int spins = 0;
  for (;;) {
    asm volatile("" ::: "memory");
    v = __builtin_amdgcn_readfirstlane(__builtin_amdgcn_raw_buffer_load_b32(rr, 0, nx_off(s), 16));
    if ((v >> 24) == (unsigned)s) break;
    if (give_up(spins, p.ws)) { dead = true; break; }
    __builtin_amdgcn_s_sleep(BSLEEP);
  }
  clock_pair(now, c0);
  return v & 0xFFFFFFu;
}
#define TL(i)
#define TL_FLUSH(role)
#define TL_DECL
#define RETRY()
#define STAMP(i)
#define STAMP_FLUSH(base, n)

template <int I> using IC = std::integral_constant<int, I>;
template <int I, int N, class F> DEV void static_for(F&& f) { if constexpr (I < N) { f(std::integral_constant<int, I>{}); static_for<I + 1, N>(f); } }
template <int NT> struct RawTile {
  unsigned voff[NT * 2];
  DEV void init(int lane, unsigned rowbytes) {
#pragma unroll
    for (int i = 0; i < NT * 2; i++) { int c = i * 64 + lane; voff[i] = (unsigned)(c / (NT * 2)) * rowbytes + (unsigned)(c % (NT * 2)) * 16u; }
  }
  DEV void issue(u32x4 srd, unsigned soff, unsigned ldsaddr) {
#pragma unroll
    for (int i = 0; i < NT * 2; i++) dma16(srd, voff[i], soff, ldsaddr + i * 1024);
  }
};
template <int NT> DEV unsigned sp_base(int lane) {
  int g = lane >> 4, q = (lane & 15) >> 2, pp = lane & 3;
  return (unsigned)((16 * (g >> 1) + 4 * (g & 1) + (q & 1) + 8 * (q >> 1)) * (NT * 32) + pp * 8);
}
template <int NT> DEV bf16x16 sp_frag(LAS char* base, int t) {
  u32x2 r0 = tr_read(base + t * 32), r1 = tr_read(base + t * 32 + 2 * NT * 32), r2 = tr_read(base + t * 32 + 32 * NT * 32), r3 = tr_read(base + t * 32 + 34 * NT * 32);
  u32x8 r = {r0.x, r0.y, r1.x, r1.y, r2.x, r2.y, r3.x, r3.y};
  return __builtin_bit_cast(bf16x16, r);
}
DEV void put_frag(LAS char* p, bf16x16 f) { u32x8 r = __builtin_bit_cast(u32x8, f); lds_w16(p, (u32x4){r[0], r[1], r[2], r[3]}); lds_w16(p + 1024, (u32x4){r[4], r[5], r[6], r[7]}); }
DEV bf16x16 get_frag(LAS char* p) { return cat(lds_r16(p), lds_r16(p + 1024)); }

constexpr int L_W = 0;
constexpr int L_X = 122880;
constexpr int L_KX = L_X, L_F = L_X + 12288;
constexpr int CHAIN_LDS = L_X + 24576;
DEV unsigned img_row(int r) { return (unsigned)(r * 32 + (r >> 3) * 128); }

#define GATHER_CHECK(v, g0, g1, N, LOADI, FAIL) do { \
    unsigned m_ = 0; _Pragma("unroll") for (int i_ = g0; i_ < g1; i_++) m_ = smax(m_, v[i_]); \
    if (__builtin_expect(any_sent(m_), 0)) { int spins_ = 0; \
      do { asm volatile("" ::: "memory"); _Pragma("unroll") for (int i_ = g0; i_ < N; i_++) { const int i = i_; v[i] = LOADI; } \
        m_ = 0; _Pragma("unroll") for (int i_ = g0; i_ < g1; i_++) m_ = smax(m_, v[i_]); PINV(m_); \
        RETRY(); if (give_up(spins_, p.ws)) { FAIL; break; } } while (any_sent(m_)); } } while (0)

DEV void await_mem(const P& p, rsrc_t rr, unsigned l16, int sl3, bool& dead) {
  int spins = 0;
#pragma unroll 1
  for (int k = 0; k < 10; k += 2) {
    for (;;) {
      asm volatile("" ::: "memory");
      const u32x4 x = ld16_dev(rr, l16, mem_off(sl3, k)), y = ld16_dev(rr, l16, mem_off(sl3, k) + 1024);
      if (!any_sent(smax(smax(0, x), y))) break;
      if (give_up(spins, p.ws)) { dead = true; return; }
    }
  }
}
DEV int rot10(int i, int r) { i += r; return i >= 10 ? i - 10 : i; }
DEV bool role_qk(const P& p, int h, LAS char* lds) {
  const int lane = threadIdx.x & 63, w = __builtin_amdgcn_readfirstlane(threadIdx.x >> 6);
  const int g = lane >> 4, col = lane & 15;
  const int rot = ROT * ((h >> 2) * 4 + w) % 10;
  bf16x16 wq[30], wk[15];
  {
    const u32x4 sq = mk_srd(p.Wq, DM * NH * DH * 2), sk = mk_srd(p.Wk, DM * NH * DH * 2);
    RawTile<3> rt; rt.init(lane, NH * DH * 2);
    const unsigned sb = (unsigned)(h * DH + w * 48) * 2u, kst = 64u * NH * DH * 2u;
    const unsigned lw = (unsigned)(size_t)(lds + L_W) + w * 30720, lx = (unsigned)(size_t)(lds + L_X) + w * 6144;
    LAS char* rb = lds + sp_base<3>(lane);
    auto slot = [&](int j) { return j < 5 ? lw + j * 6144 : lx; };
    auto slotp = [&](int j) { return rb + (j < 5 ? L_W + w * 30720 + j * 6144 : L_X + w * 6144); };
    auto issueS = [&](int i) { if (i < 10) rt.issue(sq, sb + rot10(i, rot) * kst, slot(i % 6)); else rt.issue(sk, sb + rot10((i - 10) * 2, rot) * kst, slot(i % 6)); };
    auto issueP = [&](int j) { rt.issue(sk, sb + rot10(2 * j + 1, rot) * kst, slot(j)); };
#pragma unroll
    for (int i = 0; i < 6; i++) issueS(i);
#pragma unroll
    for (int i = 0; i < 15; i++) {
      if (i < 12) WAIT_VM(30); else WAIT_VM(24);
#pragma unroll
      for (int t = 0; t < 3; t++) { bf16x16 f = sp_frag<3>(slotp(i % 6), t); if (i < 10) wq[i * 3 + t] = f; else wk[(i - 10) * 3 + t] = f; }
      WAIT_LGKM0();
      if (i < 9) issueS(i + 6); else if (i == 9) issueP(3); else if (i == 10) issueP(4); else if (i >= 12) issueP(i - 12);
    }
#pragma unroll
    for (int n = 0; n < 5; n++) {
      const int j = (n + 3) % 5;
      if (n == 0) WAIT_VM(24); else if (n == 1) WAIT_VM(18); else if (n == 2) WAIT_VM(12); else if (n == 3) WAIT_VM(6); else WAIT_VM(0);
      bf16x16 f0 = sp_frag<3>(slotp(j), 0), f1 = sp_frag<3>(slotp(j), 1), f2 = sp_frag<3>(slotp(j), 2);
      WAIT_LGKM0();
      LAS char* o = lds + L_W + w * 30720 + j * 6144 + lane * 16;
      put_frag(o, f0); put_frag(o + 2048, f1); put_frag(o + 4096, f2);
    }
    bar();
    EV(1);
  }
  bool dead = false; TL_DECL;
  const rsrc_t rf = flag_rsrc(p);
  const rsrc_t rr = mk_rsrc(ring_base(p), RING_BYTES - 8192);
  const unsigned vdl = lane == 0 ? (unsigned)(h * 128 + w * 4) : OOB;
  const rsrc_t rkm = mk_rsrc(p.ws + W_KM, NSEG * NH * 3072);
  const u32x4 skx = mk_srd(p.ws + W_KX, NSEG * NH * 12288);
  const int idx = (lane & 2) ? 0xEEEE : 0x4444;
  const unsigned l16 = lane * 16;
  LAS char* wl = lds + L_W + w * 30720 + l16;
  LAS char* fw = lds + L_F + img_row(48 * w + col) + 8 * (g ^ (col >> 2));
  const int q4 = (lane & 15) >> 2, p4 = lane & 3;
  LAS char* fr0 = lds + L_F + img_row(8 * g + q4) + 8 * (p4 ^ ((2 * g) & 3));
  LAS char* fr1 = lds + L_F + img_row(8 * g + 4 + q4) + 8 * (p4 ^ ((2 * g + 1) & 3));
  const bool qcol = (col & 2) == 0; const int mrow = 2 * (col >> 2) + (col & 1);
  const unsigned vpx = qcol ? (unsigned)((h & 15) * 512 + g * 128 + mrow * 16) : OOB;
  const unsigned vpm = (qcol && g == 0) ? (unsigned)(h * 128 + mrow * 16) : OOB;
  const unsigned vkm = qcol ? OOB : (unsigned)(g * 128 + mrow * 16);
  const unsigned lkx = (unsigned)(size_t)(lds + L_KX);
  const float cs = SCALE_LOG2E;
  auto kx_issue = [&](int s) {
#pragma unroll
    for (int i = 0; i < 3; i++) dma16_dev(skx, l16, (unsigned)((s * NH + h) * 12288 + (w * 3 + i) * 1024), lkx + (w * 3 + i) * 1024);
  };
  flag_wait<false>(p, rf, lane, FL_KX + h * 4, 1, 4, 1, dead); kx_issue(0);
  LAS char* kxr = lds + L_KX + l16; PINV(kxr); PINV(fw);
  auto steps = [&](auto W0c) {
  constexpr bool W0 = decltype(W0c)::value;
  int sl3 = 0; unsigned tn = 0;
  for (int s = 0; s < NSEG; s++) {
    f32x4 acc[6]; bf16x16 l0, l1, l2;
    auto reset = [&]() {
#pragma unroll
      for (int t = 0; t < 6; t++) { acc[t] = (f32x4){0.f, 0.f, 0.f, 0.f}; PINV(acc[t]); }
      l0 = get_frag(wl); l1 = get_frag(wl + 2048); l2 = get_frag(wl + 4096);
    };
    reset();
    unsigned so[10];
#pragma unroll
    for (int i = 0; i < 10; i++) { so[i] = mem_off(sl3, i); PINS(so[i]); }
    unsigned s_px = px_off(sl3, (h >> 4) * 8), s_pm = pm_off(sl3); PINS(s_px); PINS(s_pm);
    if (__builtin_expect(s > 0, 1)) {
      int fk = (int)__builtin_amdgcn_raw_buffer_load_b32(rf, (unsigned)(lane & 3) * 128u, (unsigned)(FL_KX + h * 4) * 128u, 16);
      unsigned now, c0; tn = next_start(p, rr, s - 1, tn, dead, now, c0);
      PINV(fk); WAIT_VM(0);
      if (s < NSEG - 1) {
        if (__builtin_expect(__builtin_amdgcn_ballot_w64(fk <= s) != 0, 0)) { flag_wait<false>(p, rf, lane, FL_KX + h * 4, 1, 4, s + 1, dead); clock_pair(now, c0); }
        kx_issue(s);
      }
      const int el = since(now, tn); if (__builtin_expect(el > 48, 0)) report_late(rr, vdl, s, now + 2); idle_from(c0, -el);
    }
    STAMP(0); TL(0);
#define MEM_LD(i) ld16_dev(rr, l16, so[i])
    auto attempt = [&]() -> int {
      u32x4 a[10]; unsigned long long bm = 0; unsigned m_;
#pragma unroll
      for (int i = 0; i < 10; i++) { a[i] = MEM_LD(i); SB(); if (i < 9) { asm volatile("s_nop " STR(GAP)); SB(); } }
      auto even = [&](auto KP) {
        constexpr int kp = KP.value, ks = 2 * kp;
        acc[0] = smf(a[ks], wq[3 * ks], acc[0], idx); SB();
        if (kp == 0) STAMP(1);
        if (kp == 0) { bm = sent_mask(a[0].x); PINS(bm); SB(); }
        if (kp == 1) { m_ = max3u(a[1].x, a[2].x, a[3].x); PINV(m_); SB(); }
        if (kp == 2) { m_ = max3u(m_, a[6].x, a[7].x); PINV(m_); SB(); }
        acc[3] = smf(a[ks], wk[3 * kp], acc[3], idx); SB();
        acc[1] = smf(a[ks], wq[3 * ks + 1], acc[1], idx); SB();
        if (kp == 2) { m_ = max3u(m_, a[8].x, a[9].x); PINV(m_); SB(); }
        acc[4] = smf(a[ks], wk[3 * kp + 1], acc[4], idx); SB();
        if (kp == 2) { bm = sent_mask(m_); PINS(bm); SB(); }
        acc[2] = smf(a[ks], wq[3 * ks + 2], acc[2], idx); SB();
        acc[5] = smf(a[ks], wk[3 * kp + 2], acc[5], idx); SB();
        if (kp == 1) { m_ = max3u(m_, a[4].x, a[5].x); PINV(m_); SB(); }
      };
      auto odd = [&](auto KP) {
        constexpr int kp = KP.value, ks = 2 * kp;
        LAS char* nx = wl + (kp + 1) * 6144;
        u32x4 n0, n1;
        acc[0] = smf(a[ks + 1], wq[3 * ks + 3], acc[0], idx); SB();
        acc[3] = smf(a[ks + 1], l0, acc[3], idx); SB();
        if (kp < 4) { n0 = lds_r16(nx); SB(); }
        acc[1] = smf(a[ks + 1], wq[3 * ks + 4], acc[1], idx); SB();
        if (kp < 4) { n1 = lds_r16(nx + 1024); l0 = cat(n0, n1); SB(); }
        acc[4] = smf(a[ks + 1], l1, acc[4], idx); SB();
        if (kp < 4) { n0 = lds_r16(nx + 2048); SB(); }
        acc[2] = smf(a[ks + 1], wq[3 * ks + 5], acc[2], idx); SB();
        if (kp < 4) { n1 = lds_r16(nx + 3072); l1 = cat(n0, n1); SB(); }
        acc[5] = smf(a[ks + 1], l2, acc[5], idx); SB();
        if (kp < 4) { l2 = get_frag(nx + 4096); SB(); }
      };
      even(IC<0>{});
      if (__builtin_expect(bm != 0, 0)) { asm volatile("" :: "v"(a[9])); return 1; }
      odd(IC<0>{}); even(IC<1>{}); odd(IC<1>{}); even(IC<2>{});
      if (__builtin_expect(bm != 0, 0)) return 1;
      odd(IC<2>{}); even(IC<3>{}); odd(IC<3>{}); even(IC<4>{}); odd(IC<4>{});
      bm = nan_mask(acc[0][0] + acc[0][2], acc[0][1] + acc[0][3]); PINS(bm); SB();
#pragma unroll
      for (int t = 0; t < 3; t++) { u32x2 v = {pk_sum(acc[t]), pk_sum(acc[3 + t])}; *(LAS u32x2*)(fw + img_row(16 * t)) = v; SB(); }
      return bm != 0 ? 2 : 0;
    };
    int miss = attempt();
    if (__builtin_expect(miss != 0, 0)) {
      for (int n = 0; miss != 0; n++) {
        RETRY();
        const bool sure = miss == 2 || n >= 3;
        if (sure) await_mem(p, rr, l16, sl3, dead);
        const unsigned t1 = rt32(), c1 = cyc32();
        report_late(rr, vdl, s, t1 + 3 + BACK / 24);
        reset();
        idle_from(c1, BACK);
        miss = attempt();
        if (sure) break;
      }
    }
    STAMP(2);
    const int nx3 = sl3 == 2 ? 0 : sl3 + 1;
    bar();
    if (W0) { if (__builtin_expect(s < NSEG - 1, 1)) {
      u32x4 kx[12], f[6];
      STAMP(3); TL(1);
      auto rd = [&](int k) { u32x2 f0 = tr_read(fr0 + k * 1536), f1 = tr_read(fr1 + k * 1536); f[k] = (u32x4){f0.x, f0.y, f1.x, f1.y}; SB(); kx[k] = lds_r16(kxr + k * 1024); SB(); kx[6 + k] = lds_r16(kxr + 6144 + k * 1024); SB(); };
      rd(0); rd(1); rd(2);
      f32x4 t0, t1, t2;
      asm volatile("s_nop 1");
      mma_v0(t0, f[0], f[0]); mma_v0(t1, kx[0], f[0]); rd(3); mma_v0(t2, kx[6], f[0]);
      mma_v(t0, f[1], f[1]); mma_v(t1, kx[1], f[1]); rd(4); mma_v(t2, kx[7], f[1]);
      mma_v(t0, f[2], f[2]); mma_v(t1, kx[2], f[2]); rd(5); mma_v(t2, kx[8], f[2]);
#pragma unroll
      for (int k = 3; k < 6; k++) { mma_v(t0, f[k], f[k]); mma_v(t1, kx[k], f[k]); mma_v(t2, kx[6 + k], f[k]); }
      asm volatile("s_nop 5" : "+v"(t0));
      float mx = fmx(t0[2], t0[3]); SB();
      asm volatile("" : "+v"(t1), "+v"(mx));
      mx = fmx3(fmx3(mx, t1[0], t1[1]), t1[2], t1[3]); SB();
      asm volatile("" : "+v"(t2), "+v"(mx));
      mx = xg_max(fmx3(fmx3(mx, t2[0], t2[1]), t2[2], t2[3]));
      STAMP(4);
      const float mc = -mx * cs;
      const f32x2 c2 = {cs, cs}, m2 = {mc, mc};
      f32x2 e[5] = {{t1[0], t1[1]}, {t1[2], t1[3]}, {t2[0], t2[1]}, {t2[2], t2[3]}, {t0[2], t0[3]}};
#pragma unroll
      for (int i = 0; i < 5; i++) { e[i] = __builtin_elementwise_fma(e[i], c2, m2); e[i][0] = __builtin_amdgcn_exp2f(e[i][0]); e[i][1] = __builtin_amdgcn_exp2f(e[i][1]); }
      const f32x2 s2 = ((e[0] + e[1]) + (e[2] + e[3])) + e[4];
      const float inv = __builtin_amdgcn_rcpf(xg_sum(s2[0] + s2[1]));
      const f32x2 i2 = {inv, inv};
      unsigned pw[5];
#pragma unroll
      for (int i = 0; i < 5; i++) pw[i] = san(__builtin_bit_cast(unsigned, __builtin_convertvector(e[i] * i2, bf16x2)));
      unsigned x0 = pw[4], x2 = x0;
      swap32(x0, x2);
      st16_dev((u32x4){pw[0], pw[1], pw[2], pw[3]}, rr, vpx, s_px);
      unsigned x1 = x0, x3 = x2; swap16(x0, x1); swap16(x2, x3);
      st16_dev((u32x4){x0, x2, x1, x3}, rr, vpm, s_pm);
      STAMP(5); TL(2);
    } } else {
      if (s == 0 && w == 1) flag_set(rf, lane, FL_RDY + h, 1);
      {
#pragma unroll
        for (int i = 0; i < 2; i++) {
          const int k = (w - 1) * 2 + i;
          u32x2 f0 = tr_read(fr0 + k * 1536), f1 = tr_read(fr1 + k * 1536);
          st16_dev((u32x4){f0.x, f0.y, f1.x, f1.y}, rkm, vkm, (unsigned)((s * NH + h) * 3072 + k * 512));
        }
        WAIT_VM(0);
      }
    }
    bar();
    if (w == 2) flag_set(rf, lane, FL_KM + h, s + 1);
    if (w == 1 && s > 0) {
        const u32x4 z = {SENT, SENT, SENT, SENT}; const int pv3 = sl3 == 0 ? 2 : sl3 - 1;
        st16_dev(z, rr, vpx, px_off(pv3, (h >> 4) * 8)); st16_dev(z, rr, vpm, pm_off(pv3));
      }
    STAMP_FLUSH(0, 9); TL_FLUSH(h);
    sl3 = nx3;
    if (dead) break;
  }
  };
  if (w == 0) steps(std::true_type{}); else steps(std::false_type{});
  return dead;
}

DEV bool role_vo(const P& p, int h, LAS char* lds) {
  const int lane = threadIdx.x & 63, w = __builtin_amdgcn_readfirstlane(threadIdx.x >> 6);
  const int g = lane >> 4, col = lane & 15;
  const int rot = ROT * ((h >> 2) * 4 + w + 5) % 10;
  bf16x16 wv[15], wo[30];
  {
    const u32x4 sv = mk_srd(p.Wv, DM * NH * DH * 2), so = mk_srd(p.Wo, DM * NH * DH * 2);
    const unsigned lw = (unsigned)(size_t)(lds + L_W) + w * 30720, lx = (unsigned)(size_t)(lds + L_X) + w * 6144;
    auto slot = [&](int j) { return j < 5 ? lw + j * 6144 : lx; };
    auto slotb = [&](int j) { return lds + (j < 5 ? L_W + w * 30720 + j * 6144 : L_X + w * 6144); };
    {
      RawTile<2> rt; rt.init(lane, DM * 2);
      const unsigned sb = (unsigned)(h * DH * DM + w * 160) * 2u; const unsigned rb = sp_base<2>(lane);
      auto issue = [&](int i) { rt.issue(so, sb + (unsigned)((i / 5) * 64 * DM + (i % 5) * 32) * 2u, slot(i % 6)); };
#pragma unroll
      for (int i = 0; i < 6; i++) issue(i);
#pragma unroll
      for (int i = 0; i < 15; i++) {
        if (i < 10) WAIT_VM(20); else if (i == 10) WAIT_VM(16); else if (i == 11) WAIT_VM(12); else if (i == 12) WAIT_VM(8); else if (i == 13) WAIT_VM(4); else WAIT_VM(0);
        wo[(i / 5) * 10 + (i % 5) * 2] = sp_frag<2>(slotb(i % 6) + rb, 0); wo[(i / 5) * 10 + (i % 5) * 2 + 1] = sp_frag<2>(slotb(i % 6) + rb, 1);
        WAIT_LGKM0();
        if (i < 9) issue(i + 6);
      }
    }
    RawTile<3> rt; rt.init(lane, NH * DH * 2);
    const unsigned sb = (unsigned)(h * DH + w * 48) * 2u, kst = 64u * NH * DH * 2u; const unsigned rb = sp_base<3>(lane);
#pragma unroll
    for (int i = 0; i < 5; i++) rt.issue(sv, sb + rot10(2 * i, rot) * kst, slot(i));
#pragma unroll
    for (int i = 0; i < 5; i++) {
      WAIT_VM(24);
#pragma unroll
      for (int t = 0; t < 3; t++) wv[i * 3 + t] = sp_frag<3>(slotb(i) + rb, t);
      WAIT_LGKM0();
      rt.issue(sv, sb + rot10(2 * i + 1, rot) * kst, slot(i));
    }
#pragma unroll
    for (int j = 0; j < 5; j++) {
      if (j == 0) WAIT_VM(24); else if (j == 1) WAIT_VM(18); else if (j == 2) WAIT_VM(12); else if (j == 3) WAIT_VM(6); else WAIT_VM(0);
      bf16x16 f0 = sp_frag<3>(slotb(j) + rb, 0), f1 = sp_frag<3>(slotb(j) + rb, 1), f2 = sp_frag<3>(slotb(j) + rb, 2);
      WAIT_LGKM0();
      LAS char* o = lds + L_W + w * 30720 + j * 6144 + lane * 16;
      put_frag(o, f0); put_frag(o + 2048, f1); put_frag(o + 4096, f2);
    }
    bar();
    EV(1);
  }
  bool dead = false; TL_DECL;
  const rsrc_t rf = flag_rsrc(p);
  const rsrc_t rr = mk_rsrc(ring_base(p), RING_BYTES - 8192);
  const unsigned vdl = lane == 0 ? (unsigned)(h * 128 + 64 + w * 4) : OOB;
  const rsrc_t ra = mk_rsrc(p.ws + W_VOM, NSEG * 40 * 8192);
  const int idx = (lane & 2) ? 0xEEEE : 0x4444;
  const unsigned l16 = lane * 16;
  LAS char* wl = lds + L_W + w * 30720 + l16;
  LAS char* vw = lds + L_X + img_row(24 * w + (col & 7)) + 8 * g + 4 * (col >> 3);
  LAS char* vr = lds + L_X + img_row(8 * g + ((lane & 15) >> 2)) + 8 * (lane & 3);
  const unsigned vst = (unsigned)(g * CH + h * 256 + col * 16), vsa = (unsigned)(g * 8192 + h * 256 + col * 16);
  const unsigned vst2 = g < 2 ? vst : OOB, vsa2 = g < 2 ? vsa : OOB;
  int sl3 = 0; unsigned tn = 0;
  for (int s = 0; s < NSEG; s++) {
    const int sl2 = s & 1;
    LAS char* vws = vw + sl2 * 4608; LAS char* vrs = vr + sl2 * 4608; PINV(vws); PINV(vrs);
    unsigned s_vo[3];
#pragma unroll
    for (int i = 0; i < 3; i++) { s_vo[i] = s < NSEG - 1 ? vo_off(sl2, 10 * w + 4 * i) : OOB; PINS(s_vo[i]); }
    f32x4 acc[3], d[10]; bf16x16 l0, l1, l2;
    auto reset = [&]() {
#pragma unroll
      for (int t = 0; t < 3; t++) { acc[t] = (f32x4){0.f, 0.f, 0.f, 0.f}; PINV(acc[t]); }
      l0 = get_frag(wl); l1 = get_frag(wl + 2048); l2 = get_frag(wl + 4096);
    };
    reset();
    unsigned so[10];
#pragma unroll
    for (int i = 0; i < 10; i++) { so[i] = mem_off(sl3, i); PINS(so[i]); }
    if (__builtin_expect(s > 0, 1)) { unsigned now, c0; tn = next_start(p, rr, s - 1, tn, dead, now, c0); WAIT_VM(0);
      const int el = since(now, tn); if (__builtin_expect(el > 48, 0)) report_late(rr, vdl, s, now + 2); idle_from(c0, -el); }
    STAMP(0); TL(0);
    auto attempt = [&]() -> int {
      u32x4 a[10]; unsigned long long bm = 0;
#pragma unroll
      for (int i = 0; i < 10; i++) { a[i] = MEM_LD(i); SB(); if (i < 9) { asm volatile("s_nop " STR(GAP)); SB(); } }
      auto pair = [&](auto KP) {
        constexpr int kp = KP.value, ks = 2 * kp;
        acc[0] = smf(a[ks], wv[3 * kp], acc[0], idx); SB();
        if (kp == 0) { bm = sent_mask(a[0].x); PINS(bm); SB(); }
        acc[1] = smf(a[ks], wv[3 * kp + 1], acc[1], idx); SB();
        acc[2] = smf(a[ks], wv[3 * kp + 2], acc[2], idx); SB();
        acc[0] = smf(a[ks + 1], l0, acc[0], idx); SB();
        if (kp < 4) { l0 = get_frag(wl + (kp + 1) * 6144); SB(); }
        acc[1] = smf(a[ks + 1], l1, acc[1], idx); SB();
        if (kp < 4) { l1 = get_frag(wl + (kp + 1) * 6144 + 2048); SB(); }
        acc[2] = smf(a[ks + 1], l2, acc[2], idx); SB();
        if (kp < 4) { l2 = get_frag(wl + (kp + 1) * 6144 + 4096); SB(); }
      };
      pair(IC<0>{});
      if (__builtin_expect(bm != 0, 0)) { asm volatile("" :: "v"(a[9])); return 1; }
      static_for<1, 5>(pair);
      bm = nan_mask(acc[0][0] + acc[0][2], acc[0][1] + acc[0][3]); PINS(bm); SB();
#pragma unroll
      for (int t = 0; t < 3; t++) { *(LAS unsigned*)(vws + img_row(8 * t)) = pk_sum(acc[t]); SB(); }
      return bm != 0 ? 2 : 0;
    };
    int miss = attempt();
    if (__builtin_expect(miss != 0, 0)) {
      for (int n = 0; miss != 0; n++) {
        RETRY();
        const bool sure = n >= 3;
        if (sure) await_mem(p, rr, l16, sl3, dead);
        const unsigned t1 = rt32(), c1 = cyc32();
        report_late(rr, vdl, s, t1 + 3 + BACK / 24);
        reset();
        idle_from(c1, BACK);
        miss = attempt();
        if (sure) break;
      }
    }
#pragma unroll
    for (int t = 0; t < 10; t++) { d[t] = (f32x4){0.f, 0.f, 0.f, 0.f}; PINV(d[t]); }
    STAMP(2);
    bar();
    u32x4 va[3];
#pragma unroll
    for (int k = 0; k < 3; k++) { u32x2 f0 = tr_read(vrs + img_row(32 * k)), f1 = tr_read(vrs + img_row(32 * k) + 128); va[k] = (u32x4){f0.x, f0.y, f1.x, f1.y}; }
    SB();
    STAMP(3);
    u32x4 o[3]; unsigned x[12];
    auto grp = [&](int t0, int n) {
#pragma unroll
      for (int k = 0; k < 3; k++)
#pragma unroll
        for (int t = t0; t < t0 + n; t++) { d[t] = smf(va[k], wo[k * 10 + t], d[t], idx); SB(); }
    };
    auto fin = [&](int tg) {
      unsigned* y = x + 4 * tg;
      swap16(y[0], y[1]); swap16(y[2], y[3]); swap32(y[0], y[2]); swap32(y[1], y[3]);
      o[tg] = (u32x4){y[0], y[1], y[2], y[3]};
      st16_dev(o[tg], rr, tg < 2 ? vst : vst2, s_vo[tg]);
    };
    if (__builtin_expect(s == 0, 0) && w == 1) flag_set(rf, lane, FL_RDY + 32 + h, 1);
    grp(0, 4);
#pragma unroll
    for (int k = 0; k < 3; k++)
#pragma unroll
      for (int t = 4; t < 8; t++) { d[t] = smf(va[k], wo[k * 10 + t], d[t], idx); SB(); if (k == 0) { x[t - 4] = san(pk_sum(d[t - 4])); SB(); } if (k == 1 && t == 4) { fin(0); SB(); } }
#pragma unroll
    for (int k = 0; k < 3; k++)
#pragma unroll
      for (int t = 8; t < 10; t++) { d[t] = smf(va[k], wo[k * 10 + t], d[t], idx); SB(); if (k < 2) { x[4 + 2 * k + t - 8] = san(pk_sum(d[4 + 2 * k + t - 8])); SB(); } }
    fin(1); SB();
    x[8] = san(pk_sum(d[8])); x[9] = san(pk_sum(d[9])); x[10] = 0; x[11] = 0;
    fin(2); SB();
    TL(2);
    STAMP(4);
#pragma unroll
    for (int tg = 0; tg < 3; tg++) st16_dev(o[tg], ra, tg < 2 ? vsa : vsa2, (unsigned)((s * 40 + 10 * w + 4 * tg) * 8192));
    if (s >= NSEG - 2) flag_set(rf, lane, FL_VOM + w * 32 + h, s);
    STAMP_FLUSH(1024, 5); TL_FLUSH(32 + h);
    sl3 = sl3 == 2 ? 0 : sl3 + 1;
    if (dead) break;
  }
  WAIT_VM(0); flag_set(rf, lane, FL_VOM + w * 32 + h, NSEG);
  return dead;
}

DEV bool role_red(const P& p, int ct, LAS char* lds) {
  const int lane = threadIdx.x & 63, w = __builtin_amdgcn_readfirstlane(threadIdx.x >> 6);
  const int g = lane >> 4, c = lane & 15;
  bool dead = false; TL_DECL;
  const rsrc_t rf = flag_rsrc(p);
  const unsigned l16 = lane * 16;
  const int r8 = RROT * (ct & 7);
  LAS unsigned* pace = (LAS unsigned*)(lds + 65536);
  const rsrc_t rr = mk_rsrc(ring_base(p), RING_BYTES - 8192);
  const int mrow = 4 * (g & 1) + 2 * (g >> 1) + (c & 1);
  const unsigned vme = (c & 6) == 0 ? (unsigned)((ct & 3) * 256 + (4 * (mrow >> 1) + 2 * (c >> 3) + (mrow & 1)) * 16) : OOB;
  if (threadIdx.x == 0) pace[2] = 0;
  if (w == 1) {
    const u32x4 z = {SENT, SENT, SENT, SENT};
    bar();
    for (int s = 1, p3 = 0; s < NSEG - 1; s++, p3 = p3 == 2 ? 0 : p3 + 1) {
      bar();
      const unsigned tn = __builtin_amdgcn_readfirstlane(pace[s & 1]); const unsigned now = rt32(), c0 = cyc32(); idle_from(c0, PRE - since(now, tn));
#pragma unroll
      for (int i = 0; i < 8; i++) st16_dev_ng(z, rr, l16, vo_off((s - 1) & 1, ct) + i * 1024);
      st16_dev(z, rr, vme, mem_off(p3, ct >> 2));
      WAIT_VM(0);
      if (lane == 0) pace[2] = s;
    }
    return false;
  }
  if (w > 1) {
    const u32x4 sx = mk_srd(p.ws + W_VOX, NSEG * 40 * NH * 1024);
    const unsigned la = (unsigned)(size_t)lds;
    int have = 0;
    for (int s = 0; s < NSEG - 1; s++) {
      if (have <= s) have = flag_wait<false>(p, rf, lane, FL_VOX + (ct / 10) * 32, 1, 32, s + 1, dead);
      if (s > 1) { const unsigned tn = __builtin_amdgcn_readfirstlane(pace[(s - 1) & 1]); const unsigned now = rt32(), c0 = cyc32(); idle_from(c0, PRED - since(now, tn)); }
      for (int i = w - 2; i < 32; i += 2) { dma16_dev(sx, l16, (unsigned)(((s * 40 + ct) * NH + (i ^ (2 * r8))) * 1024), __builtin_amdgcn_readfirstlane(la + (s & 1) * 32768 + i * 1024)); if (DSLEEP > 0 && s > 1) __builtin_amdgcn_s_sleep(DSLEEP); }
      WAIT_VM(0);
      if (dead) return true;
      bar();
    }
    return false;
  }
  const int idx = (lane & 8) ? 0xEEEE : 0x4444;
  const bool odd = c & 1;
  unsigned vo1[8], vm1[4], vm2[4];
#pragma unroll
  for (int i = 0; i < 8; i++) { vo1[i] = l16 + ((i ^ r8) << 10); PINV(vo1[i]); }
#pragma unroll
  for (int i = 0; i < 4; i++) { vm1[i] = l16 + ((i ^ (r8 & 3)) << 10); vm2[i] = l16 + ((i ^ (r8 & 3)) << 11); PINV(vm1[i]); PINV(vm2[i]); }
  int sl3 = 0, nfail = 0; unsigned tn = 0;
  for (int s = 0; s < NSEG - 1; s++) {
    if (lane == 0) pace[s & 1] = tn;
    bar();
    if (s == 0) EV(1);
    u32x4 vx[32];
#pragma unroll
    for (int i = 0; i < 32; i++) vx[i] = lds_r16(lds + (s & 1) * 32768 + i * 1024 + l16);
    const int sl2 = s & 1;
    f32x4 acc = {0.f, 0.f, 0.f, 0.f}; PINV(acc);
    u32x4 vm[8], v[20];
    const int nx3 = sl3 == 2 ? 0 : sl3 + 1; unsigned s_me = mem_off(nx3, ct >> 2); PINS(s_me);
    unsigned s_vo = vo_off(sl2, ct), s_pm = pm_off(sl3), s_p0 = px_off(sl3, 0), s_p1 = px_off(sl3, 8); PINS(s_vo); PINS(s_pm); PINS(s_p0); PINS(s_p1);
#define RED_LDM ld16_dev(rr, vm2[i >> 1] + (i & 1) * 1024, s_vo)
#define RED_LD (i < 4 ? ld16_dev(rr, vm1[i & 3], s_pm) : ld16_dev(rr, vo1[(i - 4) & 7], i < 12 ? s_p0 : s_p1))
    unsigned now, c0;
    if (__builtin_expect(s == 0, 0)) { flag_wait<false>(p, rf, lane, FL_RDY, 1, 64, 1, dead); clock_pair(now, c0); tn = now - (unsigned)((p.dr - 700) / 24); }
    else {
      clock_pair(now, c0);
      for (int n = 0; n < 64; n++) {
        idle_from(c0, DREAD - since(now, tn));
        asm volatile("" ::: "memory");
        const u32x4 v = ld16_dev(rr, (unsigned)lane * 64u, dl_off(s));
        const unsigned lo = stamp(0, s);
        int d = 0;
#pragma unroll
        for (int k = 0; k < 4; k++) if ((v[k] ^ lo) < (1u << 24)) d = max(d, tick_diff(v[k], tn));
        if (__builtin_expect(__builtin_amdgcn_ballot_w64(d > 0) == 0, 1)) break;
        d = max(d, (int)dpp<0x111>((unsigned)d)); d = max(d, (int)dpp<0x112>((unsigned)d)); d = max(d, (int)dpp<0x114>((unsigned)d)); d = max(d, (int)dpp<0x118>((unsigned)d));
        tn += (unsigned)max(max(__builtin_amdgcn_readlane(d, 15), __builtin_amdgcn_readlane(d, 31)), max(__builtin_amdgcn_readlane(d, 47), __builtin_amdgcn_readlane(d, 63)));
      }
    }
    const int el = since(now, tn);
    tn = (tn + (unsigned)p.dq) & 0xFFFFFFu;
    if (ct == 0) __builtin_amdgcn_raw_buffer_store_b32(stamp(tn, s), rr, lane < 32 ? (unsigned)lane * 4u : OOB, nx_off(s), 16);
    idle_from(c0, p.dv - el);
    TL(0);
#pragma unroll
    for (int i = 0; i < 8; i++) { vm[i] = RED_LDM; SB(); }
    idle_from(c0, p.dr - el);
    TL(1);
#pragma unroll
    for (int i = 0; i < 20; i++) { v[i] = RED_LD; SB(); }
    unsigned long long unarmed = __builtin_amdgcn_ballot_w64((int)((volatile LAS unsigned*)pace)[2] < s); PINS(unarmed);
    u32x4 vs[8]; u32x4 o; unsigned long long bad;
    auto pass = [&]() {
#pragma unroll
      for (int i = 0; i < 8; i++) { unsigned a = vm[i].x, b = vm[i].y, cc = vm[i].z, d = vm[i].w; swap16(a, b); swap16(cc, d); vs[i] = (u32x4){a, b, cc, d}; SB(); }
      STAMP(1);
#pragma unroll
      for (int i = 0; i < 20; i++) { acc = smf(v[i], i < 4 ? cat(vs[2 * i], vs[2 * i + 1]) : cat(vx[2 * (i - 4)], vx[2 * (i - 4) + 1]), acc, idx); SB(); }
      STAMP(2);
      unsigned r0 = __float_as_uint(acc[0]), r1 = __float_as_uint(acc[1]), r2 = __float_as_uint(acc[2]), r3 = __float_as_uint(acc[3]);
      swap32(r0, r2); swap32(r1, r3);
      const float u0 = __uint_as_float(r0) + __uint_as_float(r2), u1 = __uint_as_float(r1) + __uint_as_float(r3);
      bad = nan_mask(u0, u1); PINS(bad); SB();
      const float rc = __uint_as_float(dpp<0xB1>(__float_as_uint(odd ? u0 : u1)));
      const unsigned w0 = san(pk_bf16(odd ? rc : u0, odd ? u1 : rc));
      o = (u32x4){w0, dpp<0x102>(w0), dpp<0x104>(w0), dpp<0x106>(w0)};
      SB();
    };
    pass();
    if (__builtin_expect(bad != 0, 0)) {
      int spins_ = 0;
      RETRY();
      for (;;) { asm volatile("" ::: "memory");
#pragma unroll
        for (int i = 0; i < 8; i++) { vm[i] = RED_LDM; SB(); }
#pragma unroll
        for (int i = 0; i < 20; i++) { v[i] = RED_LD; SB(); }
        acc = (f32x4){0.f, 0.f, 0.f, 0.f}; PINV(acc);
        pass();
        if (bad == 0) break;
        unsigned m_ = 0;
#pragma unroll
        for (int i = 0; i < 8; i++) m_ = smax(m_, vs[i]);
#pragma unroll
        for (int i = 0; i < 20; i++) m_ = smax(m_, v[i]);
        if (!any_sent(m_)) break;
        RETRY(); if (give_up(spins_, p.ws)) { dead = true; break; }
      }
      nfail++;
    }
    STAMP(3);
    if (__builtin_expect(unarmed != 0, 0)) { int spins_ = 0; while ((int)__builtin_amdgcn_readfirstlane(((volatile LAS unsigned*)pace)[2]) < s) if (give_up(spins_, p.ws)) { dead = true; break; } }
    st16_dev(o, rr, vme, s_me);
    SB(); TL(2);
    if (s == 0) EV(6);
    STAMP(4);
    STAMP_FLUSH(2048, 5); TL_FLUSH(64 + ct);
    sl3 = nx3;
    if (dead) break;
  }
  return dead;
}
#define OWARM 6
#define OWS 0
#define WARM2 1
#define EAGER 3
#define OFREE 6
#define SSLEEP 0
#define OSLEEP 0
#define INSL 4
#define WARM 0
#define QDELAY 8000
#define WS_DECL
#define WS(i)
#define WS_FLUSH(role, on)
template <int NT> DEV unsigned dn_base(int lane) { return (unsigned)((8 * (lane >> 4) + ((lane & 15) >> 2)) * (NT * 32) + (lane & 3) * 8); }
template <int NT> DEV u32x4 dn_frag(LAS char* base, int j, int t) {
  u32x2 r0 = tr_read(base + 32 * j * NT * 32 + t * 32), r1 = tr_read(base + (32 * j + 4) * NT * 32 + t * 32);
  return (u32x4){r0.x, r0.y, r1.x, r1.y};
}
template <int N> DEV void wait_vm_c() { asm volatile("s_waitcnt vmcnt(%0)" :: "n"(N) : "memory"); }
template <int NT, int NST, int NSL, class SO, class PUT>
DEV void load_weights(const void* W, unsigned bytes, unsigned rowbytes, LAS char* stage, int lane, SO soff, PUT put) {
  const u32x4 srd = mk_srd(W, bytes);
  RawTile<NT> rt; rt.init(lane, rowbytes);
  const unsigned la = (unsigned)(size_t)stage; LAS char* rb = stage + dn_base<NT>(lane);
  static_for<0, NSL>([&](auto I) { rt.issue(srd, soff(I.value), la + I.value * NT * 2048); });
  static_for<0, NST>([&](auto I) {
    constexpr int i = I.value, left = (NST - 1 - i < NSL - 1 ? NST - 1 - i : NSL - 1);
    wait_vm_c<left * NT * 2>();
#pragma unroll
    for (int j = 0; j < 2; j++)
#pragma unroll
      for (int t = 0; t < NT; t++) put(i, j, t, dn_frag<NT>(rb + (i % NSL) * NT * 2048, j, t));
    WAIT_LGKM0();
    if constexpr (i + NSL < NST) rt.issue(srd, soff(i + NSL), la + (i % NSL) * NT * 2048);
  });
}
template <int MIDK, class PREF, class GAPF, class MID>
DEV void gemm640(const u32x4 (&wf)[60], f32x4 (&acc)[3][2], LAS char* xb, PREF pre, GAPF gap, MID mid) {
  u32x4 b0 = lds_r16(xb), b1 = lds_r16(xb + 1024);
  static_for<0, 20>([&](auto KS) {
    constexpr int ks = KS.value;
    u32x4 n0, n1;
    auto mm = [&](int t, int T) { if (ks == 0) mma_w0(acc[t][T], wf[t], T ? b1 : b0); else mma_w(acc[t][T], wf[ks * 3 + t], T ? b1 : b0); SB(); };
    mm(0, 0);
    if (ks < 19) { n0 = lds_r16(xb + (ks + 1) * 2048); SB(); }
    mm(0, 1);
    if (ks < 19) { n1 = lds_r16(xb + (ks + 1) * 2048 + 1024); SB(); }
    mm(1, 0);
    pre(KS); SB();
    mm(1, 1);
    gap(KS); SB();
    mm(2, 0); mm(2, 1);
    if (ks == MIDK) mid();
    b0 = n0; b1 = n1;
  });
  asm volatile("s_nop 7" : "+v"(acc[0][0]), "+v"(acc[0][1]), "+v"(acc[1][0]), "+v"(acc[1][1]), "+v"(acc[2][0]), "+v"(acc[2][1]));
}
template <int D> DEV void tile_pre(unsigned ldsaddr) { if constexpr (D >= 0 && D < 10 && (D & 3) == 0) dma_base(ldsaddr + D * 1024); }
template <int D, bool DEVSCOPE> DEV void tile_dma(u32x4 srd, unsigned voff, unsigned soff) { if constexpr (D >= 0 && D < 10) dma_at<(D & 3) * 1024, DEVSCOPE>(srd, voff, soff + (D & ~3) * 1024); }
DEV u32x4 chunk_bf16(f32x4 a, f32x4 b) {
  unsigned x0 = pk_bf16(a[0], a[1]), x1 = pk_bf16(a[2], a[3]), y0 = pk_bf16(b[0], b[1]), y1 = pk_bf16(b[2], b[3]);
  swap16(x0, y0); swap16(x1, y1); return (u32x4){x0, x1, y0, y1};
}
DEV void chunk_f32(f32x4& a, f32x4& b) {
#pragma unroll
  for (int i = 0; i < 4; i++) { unsigned x = __float_as_uint(a[i]), y = __float_as_uint(b[i]); swap16(x, y); a[i] = __uint_as_float(x); b[i] = __uint_as_float(y); }
}
DEV float bf_lo(unsigned u) { return __uint_as_float(u << 16); }
DEV float bf_hi(unsigned u) { return __uint_as_float(u & 0xFFFF0000u); }
constexpr int L_STG = 40960, L_QF = 81920, L_KQ = L_QF + 12288;
template <int NSL>
DEV void load_w640(u32x4 (&wf)[60], const void* W, unsigned bytes, unsigned rowbytes, unsigned col0, LAS char* lds, int lane, int w) {
  load_weights<3, 10, NSL>(W, bytes, rowbytes, lds + L_STG + w * 24576, lane, [&](int i) { return (col0 + w * 48) * 2u + (unsigned)i * 64u * rowbytes; },
                         [&](int i, int j, int t, u32x4 f) { wf[(2 * i + j) * 3 + t] = f; });
}
DEV int pos_chain(int j) { return ((j >> 3) & 1) * 6144 + (4 * (2 * (j >> 4) + ((j >> 1) & 1)) + 2 * ((j >> 2) & 1) + (j & 1)) * 16; }
DEV int pos_nat(int j) { return ((j >> 2) & 1) * 6144 + (4 * (j >> 3) + (j & 3)) * 16; }

DEV void warm_rows(const void* W, unsigned col0, unsigned rowbytes, int row0, int nrows, int rb16) {
  const rsrc_t r = mk_rsrc(W, DM * NH * DH * 2);
  const int n = nrows * rb16;
#pragma unroll 1
  for (int c0 = 0; c0 < n; c0 += 256 * 8) {
    u32x4 v[8];
#pragma unroll
    for (int i = 0; i < 8; i++) { const int c = c0 + i * 256 + (int)threadIdx.x; v[i] = ld16(r, c < n ? (unsigned)(row0 + c / rb16) * rowbytes + col0 + (unsigned)(c % rb16) * 16u : OOB, 0); }
    asm volatile("" :: "v"(v[0]), "v"(v[1]), "v"(v[2]), "v"(v[3]), "v"(v[4]), "v"(v[5]), "v"(v[6]), "v"(v[7]));
  }
}
template <int KIND>
DEV bool role_proj(const P& p, int h, LAS char* lds) {
  const int lane = threadIdx.x & 63, w = __builtin_amdgcn_readfirstlane(threadIdx.x >> 6);
  const int g = lane >> 4, col = lane & 15; const unsigned l16 = lane * 16;
  const u32x4 sx = mk_srd(p.ws + W_XN, NSEG * 40960);
  const unsigned la = (unsigned)(size_t)lds;
#pragma unroll
  for (int i = 0; i < 10; i++) dma16(sx, l16, (unsigned)((w * 10 + i) * 1024), la + (w * 10 + i) * 1024);
  u32x4 wf[60];
  if (KIND == 2 && WARM) { warm_rows(p.Wv, h * DH * 2, NH * DH * 2, 384, 256, 24); warm_rows(p.Wo, 0, DM * 2, h * DH + 128, 64, 80); warm_rows(p.Wk, h * DH * 2, NH * DH * 2, 384, 256, 24); }
  load_w640<4>(wf, KIND == 0 ? p.Wk : KIND == 1 ? p.Wv : p.Wq, DM * NH * DH * 2, NH * DH * 2, h * DH, lds, lane, w);
  EV(1);
  const rsrc_t rf = flag_rsrc(p);
  const rsrc_t ro = mk_rsrc(p.ws + (KIND == 1 ? W_V : W_KX), NSEG * NH * 12288);
  const int j = 16 * (g & 1) + col;
  unsigned vo[3];
#pragma unroll
  for (int t = 0; t < 3; t++) { const int et = 3 * w + t; vo[t] = (unsigned)((KIND == 2 ? (g & 1) * 6144 + col * 16 : KIND == 0 ? pos_chain(j) : pos_nat(j)) + (et >> 1) * 1024 + (2 * (et & 1) + (g >> 1)) * 256); }
  const rsrc_t rkm = mk_rsrc(p.ws + W_KM, NSEG * NH * 3072), rpx = mk_rsrc(p.ws + W_PX, NSEG * 2 * NH * 1024), rpm = mk_rsrc(p.ws + W_PM, NSEG * 16 * 1024);
  const unsigned vk0 = (unsigned)(pos_chain(8 * (col >> 2) + (col & 3)) + g * 256), vk1 = (unsigned)(pos_chain(8 * (col >> 2) + 4 + (col & 3)) + g * 256);
  const u32x4 skx = mk_srd(p.ws + W_KX, NSEG * NH * 12288), sfl = mk_srd(p.ws + W_FLAG, 163840);
  const unsigned vkm = (unsigned)(g * 128 + (col & 7) * 16);
  LAS char* qb = lds + L_QF + l16; PINV(qb);
  const unsigned vkw = w < 2 ? vk0 : vk1;
  const u32x4 swarm = KIND == 0 ? mk_srd(p.w_in, DM * FF * 2) : mk_srd(p.w_sp, SEQ * SEQ * 2);
  const int qi = 16 * w + col - 8 * g;
  bool dead = false, pend = false; int kx_have = 0; WS_DECL;
  if (KIND == 2) {
    bar();
    kx_have = flag_wait(p, rf, lane, FL_KX + h * 4, 1, 4, 1, dead);
#pragma unroll
    for (int i = 0; i < 3; i++) dma16_dev(skx, vkw, (unsigned)(h * 12288 + ((w & 1) * 3 + i) * 1024), la + L_KQ + (w * 3 + i) * 1024);
  }
  for (int s = 0; s < NSEG; s++) {
    WS(0);
    const int sn = s + 1 < NSEG ? s + 1 : s;
    unsigned l_x = la + ((s + 1) & 1) * 40960 + w * 10240, s_x = (unsigned)(sn * 40960 + w * 10240), l_k = la + L_KQ + ((s + 1) & 1) * 12288 + w * 3072, s_k = (unsigned)((sn * NH + h) * 12288 + (w & 1) * 3072);
    unsigned s_m = (unsigned)((s * NH + h) * 3072);
    constexpr int WN = KIND == 0 ? 8 : 32, W0 = 60 - WN;
    unsigned s_w = WARM2 && s >= W0 && s < 60 ? (unsigned)(((h * 4 + w) * WN + s - W0) * 2048) : OOB; if (KIND != 2) PINS(s_w);
    PINS(l_x); PINS(s_x); if (KIND == 2) { PINS(l_k); PINS(s_k); PINS(s_m); }
    LAS char* kb = qb + 12288 + (s & 1) * 12288; LAS char* xb = lds + (s & 1) * 40960 + l16; PINV(xb); if (KIND == 2) PINV(kb);
    if (KIND == 2 && kx_have <= sn) kx_have = flag_wait<false>(p, rf, lane, FL_KX + h * 4, 1, 4, sn + 1, dead);
    if (KIND != 2) { if (s < 2) WAIT_VM(0); }
    else if (s == 0) WAIT_VM(0);
    bar();
    WS(1);
    bool have = false;
    u32x4 km[6], kx[12];
    auto km_issue = [&](unsigned so) {
#pragma unroll
      for (int k = 0; k < 6; k++) km[k] = ld16_dev(rkm, vkm + k * 512, so);
      SB();
    };
    int fm = 0;
    f32x4 acc[3][2];
    WS(3);
    gemm640<KIND == 2 ? 7 : 14>(wf, acc, xb,
            [&](auto KS) { tile_pre<KS.value - (KIND == 2 ? 4 : 0)>(l_x); if (KIND == 2 && KS.value == 1) dma_base(l_k); if (KIND != 2 && KS.value == 10) dma_base(la + L_QF + w * 2048); },
            [&](auto KS) {
              constexpr int ks = KS.value;
              tile_dma<ks - (KIND == 2 ? 4 : 0), false>(sx, l16, s_x);
              if constexpr (KIND != 2 && (ks == 10 || ks == 11)) dma_at<(ks - 10) * 1024, false>(swarm, l16, s_w);
              if constexpr (KIND == 2 && ks >= 1 && ks < 4) dma_at<(ks - 1) * 1024, true>(skx, vkw, s_k);
              if (KIND == 2 && ks == 0) asm volatile("buffer_load_dword %0, off, %1, %2 sc1" : "=v"(fm) : "s"(sfl), "s"((unsigned)(FL_KM + h) * 128u) : "memory");
              if constexpr (KIND == 2 && ks >= 14) { kx[2 * (ks - 14)] = lds_r16(kb + 2 * (ks - 14) * 1024); kx[2 * (ks - 14) + 1] = lds_r16(kb + (2 * (ks - 14) + 1) * 1024); }
            },
            [&]() { WS(4); if (KIND != 2) { if (s > 0) WAIT_VM(0); if (s > EAGER) flag_set(rf, lane, (KIND == 0 ? FL_KX : FL_V) + h * 4 + w, s); } else {
              asm volatile("s_waitcnt vmcnt(7)" : "+v"(fm) :: "memory");
              if (pend) flag_set(rf, lane, FL_PX + w * 32 + h, s);
              have = w < 2 && __builtin_amdgcn_readfirstlane(fm) > s;
              km_issue(have ? s_m : OOB); } WS(5); });
    WS(6);
    if (KIND != 2) {
#pragma unroll
      for (int t = 0; t < 3; t++) st16_dev(chunk_bf16(acc[t][0], acc[t][1]), ro, vo[t], (unsigned)((s * NH + h) * 12288));
      if (s < EAGER) { WAIT_VM(0); flag_set(rf, lane, (KIND == 0 ? FL_KX : FL_V) + h * 4 + w, s + 1); if (s == 0) EV(6); }
    } else {
#pragma unroll
      for (int t = 0; t < 3; t++) lds_w16(lds + L_QF + vo[t], chunk_bf16(acc[t][0], acc[t][1]));
      asm volatile("" :: "v"(km[0]), "v"(km[1]), "v"(km[2]), "v"(km[3]), "v"(km[4]), "v"(km[5]));
      WAIT_VM(0);
      bar();
      if (w < 2) {
        u32x4 q[6];
        f32x4 t0, t1, t2;
#pragma unroll
        for (int k = 0; k < 6; k++) { q[k] = lds_r16(qb + w * 6144 + k * 1024); SB(); }
        mma_v0(t1, kx[0], q[0]); mma_v0(t2, kx[6], q[0]);
#pragma unroll
        for (int k = 1; k < 6; k++) { mma_v(t1, kx[k], q[k]); mma_v(t2, kx[6 + k], q[k]); }
        WS(7);
        if (!have) { flag_wait<false>(p, rf, lane, FL_KM + h, 1, 1, s + 1, dead); km_issue(s_m); }
        WS(8);
        asm volatile("s_nop 1");
        mma_v0(t0, km[0], q[0]);
#pragma unroll
        for (int k = 1; k < 6; k++) mma_v(t0, km[k], q[k]);
        asm volatile("s_nop 1" : "+v"(t1), "+v"(t2));
        const float ninf = -__builtin_inff();
#pragma unroll
        for (int i = 0; i < 4; i++) { if (i > qi) t1[i] = ninf; if (4 + i > qi) t2[i] = ninf; }
        float mx = fmx3(fmx3(t1[0], t1[1], t1[2]), t1[3], fmx3(fmx3(t2[0], t2[1], t2[2]), t2[3], t2[3]));
        SB();
        asm volatile("" : "+v"(t0), "+v"(mx));
        mx = xg_max(fmx3(fmx3(mx, t0[0], t0[1]), t0[2], t0[3]));
        const float mc = -mx * SCALE_LOG2E;
        const f32x4 c4 = {SCALE_LOG2E, SCALE_LOG2E, SCALE_LOG2E, SCALE_LOG2E}, m4 = {mc, mc, mc, mc};
        t0 = __builtin_elementwise_fma(t0, c4, m4); t1 = __builtin_elementwise_fma(t1, c4, m4); t2 = __builtin_elementwise_fma(t2, c4, m4);
#pragma unroll
        for (int i = 0; i < 4; i++) { t0[i] = __builtin_amdgcn_exp2f(t0[i]); t1[i] = __builtin_amdgcn_exp2f(t1[i]); t2[i] = __builtin_amdgcn_exp2f(t2[i]); }
        const f32x4 s4 = __builtin_elementwise_fma(t0, (f32x4){0.5f, 0.5f, 0.5f, 0.5f}, t1 + t2);
        const float inv = __builtin_amdgcn_rcpf(xg_sum((s4[0] + s4[1]) + (s4[2] + s4[3])));
        t0 *= inv; t1 *= inv; t2 *= inv;
        st16_dev((u32x4){pk_bf16(t1[0], t1[1]), pk_bf16(t1[2], t1[3]), pk_bf16(t2[0], t2[1]), pk_bf16(t2[2], t2[3])}, rpx, l16, (unsigned)(((s * 2 + w) * NH + h) * 1024));
        unsigned x0 = pk_bf16(t0[0], t0[1]), x1 = pk_bf16(t0[2], t0[3]), y0 = x0, y1 = x1;
        swap16(x0, y0); swap16(x1, y1);
        st16_dev((u32x4){x0, x1, y0, y1}, rpm, g == 0 ? l16 : OOB, (unsigned)(((s * 2 + w) * 8 + (h >> 2)) * 1024 + (h & 3) * 256));
        pend = s + 1 < NSEG;
        if (!pend) { WAIT_VM(0); flag_set(rf, lane, FL_PX + w * 32 + h, s + 1); }
      }
    }
    WS(9); WS_FLUSH(KIND, h == 0);
    if (dead) return true;
  }
  if (KIND != 2) { WAIT_VM(0); flag_set(rf, lane, (KIND == 0 ? FL_KX : FL_V) + h * 4 + w, NSEG); }
  return false;
}

#define ODMA dma16_dev
#define OFLAG 1
DEV bool role_o(const P& p, int h, LAS char* lds) {
  const int lane = threadIdx.x & 63, w = __builtin_amdgcn_readfirstlane(threadIdx.x >> 6);
  const unsigned l16 = lane * 16;
  u32x4 wf[60];
  load_weights<2, 15, 6>(p.Wo, DM * NH * DH * 2, DM * 2, lds + 24576 + w * 24576, lane,
                         [&](int i) { return (unsigned)(h * DH * DM + w * 160 + (i / 5) * 64 * DM + (i % 5) * 32) * 2u; },
                         [&](int i, int j, int t, u32x4 f) { wf[(2 * (i / 5) + j) * 10 + (i % 5) * 2 + t] = f; });
  EV(1);
  const rsrc_t rf = flag_rsrc(p), ro = mk_rsrc(p.ws + W_VOX, NSEG * 40 * NH * 1024);
  const u32x4 sv = mk_srd(p.ws + W_V, NSEG * NH * 12288);
  const unsigned la = (unsigned)(size_t)lds;
  bool dead = false; int have = 0;
  auto v_dma = [&](int s) {
#pragma unroll
    for (int i = 0; i < 3; i++) ODMA(sv, l16, (unsigned)((s * NH + h) * 12288 + (w * 3 + i) * 1024), la + (s & 1) * 12288 + (w * 3 + i) * 1024);
  };
  have = flag_wait<false>(p, rf, lane, FL_V + h * 4, 1, 4, 1, dead); v_dma(0);
  bool late = true; WS_DECL;
  for (int s = 0; s < NSEG; s++) {
    WS(0);
    if (late) WAIT_VM(0); else WAIT_VM(8);
    bar();
    WS(1);
    LAS char* vb = lds + (s & 1) * 12288 + l16;
    u32x4 a[12];
#pragma unroll
    for (int k = 0; k < 6; k++) { a[k] = lds_r16(vb + k * 1024); a[6 + k] = lds_r16(vb + 6144 + k * 1024); }
    int fv = (int)__builtin_amdgcn_raw_buffer_load_b32(rf, (unsigned)(lane & 3) * 128u, (unsigned)(FL_V + h * 4) * 128u, 16);
    f32x4 c[2][2]; u32x4 o;
#define O_AUX 16
    auto put = [&](int t) { if (O_AUX >= 0) __builtin_amdgcn_raw_buffer_store_b128(o, ro, l16, (unsigned)(((s * 40 + 10 * w + t) * NH + h) * 1024), O_AUX < 0 ? 0 : O_AUX); st_guard(o); };
    auto cvt = [&](int t, int i) { f32x4& d = c[t & 1][i >> 1]; o[i] = pk_bf16(d[2 * (i & 1)], d[2 * (i & 1) + 1]); };
    asm volatile("s_nop 1");
#pragma unroll
    for (int t = 0; t < 10; t++) {
      f32x4& c0 = c[t & 1][0]; f32x4& c1 = c[t & 1][1];
#pragma unroll
      for (int k = 0; k < 6; k++) {
        if (t > 0 && k == 4) asm volatile("" : "+v"(c[(t - 1) & 1][0]), "+v"(c[(t - 1) & 1][1]));
        if (k == 0) mma_a0(c0, a[k], wf[k * 10 + t]); else mma_a(c0, a[k], wf[k * 10 + t]);
        SB();
        if (t > 0 && k == 4) { cvt(t - 1, 0); cvt(t - 1, 1); SB(); }
        if (t > 0 && k == 5) { put(t - 1); SB(); }
        if (k == 0) mma_a0(c1, a[6 + k], wf[k * 10 + t]); else mma_a(c1, a[6 + k], wf[k * 10 + t]);
        SB();
        if (t > 0 && k == 4) { cvt(t - 1, 2); cvt(t - 1, 3); SB(); }
      }
      if (t == 2) {
        WAIT_VM(2); PINV(fv);
        late = s + 1 < NSEG && __builtin_amdgcn_ballot_w64(fv < s + 2) != 0;
        SB();
        if (s > 0 && (OFLAG || (s & 7) == 0)) flag_set(rf, lane, FL_VOX + w * 32 + h, s);
        if (!late && s + 1 < NSEG) v_dma(s + 1);
        SB();
      }
      if (t >= 1 && t <= 8) WS(t + 1);
    }
    asm volatile("s_nop 7" : "+v"(c[1][0]), "+v"(c[1][1]));
    cvt(9, 0); cvt(9, 1); cvt(9, 2); cvt(9, 3); put(9);
    if (s < EAGER) { WAIT_VM(0); flag_set(rf, lane, FL_VOX + w * 32 + h, s + 1); if (s == 0) EV(6); }
    if (late) { flag_wait<false>(p, rf, lane, FL_V + h * 4, 1, 4, s + 2, dead); v_dma(s + 1); }
    WS_FLUSH(3, h == 0);
    if (dead) return true;
  }
  WAIT_VM(0); flag_set(rf, lane, FL_VOX + w * 32 + h, NSEG);
  return false;
}

DEV bool role_out(const P& p, int xq, int gi, int hf, LAS char* lds) {
  const int lane = threadIdx.x & 63, w = __builtin_amdgcn_readfirstlane(threadIdx.x >> 6);
  const int g = lane >> 4, col = lane & 15; const unsigned l16 = lane * 16;
  const int ct0 = 10 * xq + 5 * hf;
  const rsrc_t rf = flag_rsrc(p), rvx = mk_rsrc(p.ws + W_VOX, NSEG * 40 * NH * 1024), rvm = mk_rsrc(p.ws + W_VOM, NSEG * 40 * 8192);
  const rsrc_t rpx = mk_rsrc(p.ws + W_PX, NSEG * 2 * NH * 1024), rpm = mk_rsrc(p.ws + W_PM, NSEG * 16 * 1024);
  const rsrc_t rx = mk_rsrc(p.x, SEQ * DM * 2), rh = mk_rsrc(p.ws + W_H, NSEG * 40960);
  const rsrc_t rr = mk_rsrc(ring_base(p), RING_BYTES - 8192);
  const u32x4 svx = mk_srd(p.ws + W_VOX, NSEG * 40 * NH * 1024);
  const unsigned vxo = (unsigned)(((16 * (g & 1) + col) * DM + 8 * (g >> 1)) * 2), vho = (unsigned)((g & 1) * 1024 + (g >> 1) * 256 + col * 16);
  bool dead = false; int have = 0, par = 0;
  for (int s = gi; s < NSEG; s += 3, par ^= 1) {
    if (have <= s) have = flag_wait(p, rf, lane, FL_VOX + xq * 32 + 8 * w, 1, 8, s + 1, dead);
    auto reached = [&](int t) { asm volatile("" ::: "memory"); return (__builtin_amdgcn_readfirstlane(__builtin_amdgcn_raw_buffer_load_b32(rr, 0, nx_off(t), 16)) >> 24) == (unsigned)t; };
    auto await = [&](int t) { int spins = 0; while (!reached(t)) { if (give_up(spins, p.ws)) { dead = true; break; } __builtin_amdgcn_s_sleep(8); } };
    const bool behind = s < NSEG - 3 && reached(s + 2);
    if (!behind && s < NSEG - 1) await(s);
    auto calm = [&]() { if (OSLEEP > 0) { SB(); if (s < NSEG - OFREE) __builtin_amdgcn_s_sleep(OSLEEP); SB(); } };
    u32x4 vx[8][5], xr[2];
#pragma unroll
    for (int i = 0; i < 8; i++)
#pragma unroll
      for (int t = 0; t < 5; t++) { vx[i][t] = ld16_dev(rvx, l16, (unsigned)(((s * 40 + ct0 + t) * NH + 8 * w + i) * 1024)); calm(); }
    xr[0] = ld16(rx, vxo, (unsigned)((s * 32 * DM + (ct0 + w) * 16) * 2)); xr[1] = ld16(rx, vxo, (unsigned)((s * 32 * DM + (ct0 + 4) * 16) * 2));
    SB();
    { int spins = 0;
      for (;;) { asm volatile("" ::: "memory");
        const int v = (int)__builtin_amdgcn_raw_buffer_load_b32(rf, (unsigned)((lane & 7) + ((lane >> 3) & 1) * 32) * 128u, (unsigned)(FL_PX + 8 * w) * 128u, 16);
        if (__builtin_amdgcn_ballot_w64(v < s + 1) == 0) break;
        if (give_up(spins, p.ws)) { dead = true; break; }
        if (s < NSEG - 3) __builtin_amdgcn_s_sleep(8); } }
    if (s >= NSEG - 3) flag_wait<false>(p, rf, lane, FL_VOM + xq * 32 + 8 * w, 1, 8, s + 1, dead); else if (!behind) await(s + 2);
    u32x4 vm[2][5], px[10][2];
#pragma unroll
    for (int i = 0; i < 10; i++)
#pragma unroll
      for (int T = 0; T < 2; T++) { px[i][T] = i < 8 ? ld16_dev(rpx, l16, (unsigned)(((s * 2 + T) * NH + 8 * w + i) * 1024)) : ld16_dev(rpm, l16, (unsigned)(((s * 2 + T) * 8 + 2 * w + i - 8) * 1024)); SB(); calm(); }
#pragma unroll
    for (int i = 0; i < 2; i++)
#pragma unroll
      for (int t = 0; t < 5; t++) { vm[i][t] = ld16_dev(rvm, l16, (unsigned)((s * 40 + ct0 + t) * 8192 + (2 * w + i) * 1024)); calm(); }
    SB();
    f32x4 acc[5][2] = {};
#pragma unroll
    for (int i = 0; i < 10; i++) {
#pragma unroll
      for (int t = 0; t < 5; t++) { const u32x4 a = i < 8 ? vx[i][t] : vm[i - 8][t]; acc[t][0] = mma(a, px[i][0], acc[t][0]); acc[t][1] = mma(a, px[i][1], acc[t][1]); }
      SB();
    }
    LAS char* red = lds + par * 40960 + l16;
#pragma unroll
    for (int t = 0; t < 5; t++) { *(LAS f32x4*)(red + (w * 10 + t * 2) * 1024) = acc[t][0]; *(LAS f32x4*)(red + (w * 10 + t * 2 + 1) * 1024) = acc[t][1]; }
    bar();
    auto fin = [&](int t, u32x4 xv) {
      f32x4 a = *(LAS f32x4*)(red + t * 2048), b = *(LAS f32x4*)(red + t * 2048 + 1024);
#pragma unroll
      for (int i = 1; i < 4; i++) { a += *(LAS f32x4*)(red + i * 10240 + t * 2048); b += *(LAS f32x4*)(red + i * 10240 + t * 2048 + 1024); }
      chunk_f32(a, b);
      const int ct = ct0 + t;
      st16_dev((u32x4){pk_bf16(a[0] + bf_lo(xv.x), a[1] + bf_hi(xv.x)), pk_bf16(a[2] + bf_lo(xv.y), a[3] + bf_hi(xv.y)), pk_bf16(b[0] + bf_lo(xv.z), b[1] + bf_hi(xv.z)), pk_bf16(b[2] + bf_lo(xv.w), b[3] + bf_hi(xv.w))},
               rh, vho, (unsigned)(s * 40960 + (ct >> 1) * 2048 + (ct & 1) * 512));
    };
    fin(w, xr[0]);
    if (w == 0) fin(4, xr[1]);
    WAIT_VM(0);
    flag_set(rf, lane, FL_H + gi * 32 + (xq * 2 + hf) * 4 + w, s + 1);
    if (OWARM && s + OWARM < NSEG - 1) {
      if (have <= s + OWARM) have = max(have, flag_wait<false>(p, rf, lane, FL_VOX + xq * 32 + 8 * w, 1, 8, s + 1, dead));
      if (have > s + OWARM) {
        dma_base((unsigned)(size_t)lds + 81920 + w * 4096);
#pragma unroll 1
        for (int t = 0; t < 5; t++)
#pragma unroll
          for (int i = 0; i < 2; i++) { const unsigned so = (unsigned)((((s + OWARM) * 40 + ct0 + t) * NH + 8 * w + 4 * i) * 1024);
            dma_at<0, true>(svx, l16, so); dma_at<1024, true>(svx, l16, so); dma_at<2048, true>(svx, l16, so); dma_at<3072, true>(svx, l16, so); if (OWS > 0) __builtin_amdgcn_s_sleep(OWS); }
      }
    }
    if (dead) return true;
  }
  WAIT_VM(0);
  return false;
}

DEV f32x4 gelu_t(f32x4 x) {
  const f32x4 c1 = {-0.10294324f, -0.10294324f, -0.10294324f, -0.10294324f}, c0 = {-2.3022082f, -2.3022082f, -2.3022082f, -2.3022082f};
  f32x4 u = x * __builtin_elementwise_fma(x * x, c1, c0);
#pragma unroll
  for (int i = 0; i < 4; i++) u[i] = __builtin_amdgcn_exp2f(u[i]);
  u += 1.0f;
#pragma unroll
  for (int i = 0; i < 4; i++) u[i] = __builtin_amdgcn_rcpf(u[i]);
  return x * u;
}
DEV bool role_in(const P& p, int gi, int sl, LAS char* lds) {
  const int lane = threadIdx.x & 63, w = __builtin_amdgcn_readfirstlane(threadIdx.x >> 6);
  const int g = lane >> 4, col = lane & 15; const unsigned l16 = lane * 16;
  bar();
  u32x4 wf[60];
  load_w640<INSL>(wf, p.w_in, DM * FF * 2, FF * 2, sl * 192, lds, lane, w);
  u32x4 ones = {0x3F803F80u, 0x3F803F80u, 0x3F803F80u, 0x3F803F80u};
  PINV(ones);
  f32x4 cs[3];
  asm volatile("s_nop 1");
#pragma unroll
  for (int ks = 0; ks < 20; ks++)
#pragma unroll
    for (int t = 0; t < 3; t++) { if (ks == 0) mma_w0(cs[t], wf[t], ones); else mma_w(cs[t], wf[ks * 3 + t], ones); }
  asm volatile("s_nop 7" : "+v"(cs[0]), "+v"(cs[1]), "+v"(cs[2]));
  const rsrc_t rf = flag_rsrc(p), rz = mk_rsrc(p.ws + (sl < 4 ? W_Z1 : W_Z2), NSEG * 49152), rs = mk_rsrc(p.ws + W_STAT, 65536);
  const u32x4 sh = mk_srd(p.ws + W_H, NSEG * 40960);
  const unsigned la = (unsigned)(size_t)lds;
  LAS float* st = (LAS float*)(lds + 81920);
  LAS char* zi = lds + 86016 + w * 4096;
  bool dead = false, pend = false; int par = 0; WS_DECL;
  f32x4 z[3][2]; int sp = -1;
  auto h_dma = [&](int s, int buf, int i) { dma16_dev(sh, l16, (unsigned)(s * 40960 + (w * 10 + i) * 1024), la + buf * 40960 + (w * 10 + i) * 1024); };
  u32x2 o[2][4] = {};
  auto st_load = [&]() {
    asm volatile("" ::: "memory");
#pragma unroll
    for (int T = 0; T < 2; T++)
#pragma unroll
      for (int i = 0; i < 4; i++) o[T][i] = __builtin_amdgcn_raw_buffer_load_b64(rs, (unsigned)((T * 16 + col) * 8), (unsigned)((sp * 4 + i) * 256), 16);
  };
  auto finish = [&]() {
    int spins = 0;
    for (;;) { unsigned m_ = 0;
#pragma unroll
      for (int T = 0; T < 2; T++)
#pragma unroll
        for (int i = 0; i < 4; i++) m_ = max3u(m_, o[T][i].x, o[T][i].y);
      if (!any_sent(m_)) break;
      if (give_up(spins, p.ws)) { dead = true; break; }
      st_load(); }
#pragma unroll
    for (int T = 0; T < 2; T++) {
      const float mu = 0.25f * ((__uint_as_float(o[T][0].x) + __uint_as_float(o[T][1].x)) + (__uint_as_float(o[T][2].x) + __uint_as_float(o[T][3].x)));
      float m2 = (__uint_as_float(o[T][0].y) + __uint_as_float(o[T][1].y)) + (__uint_as_float(o[T][2].y) + __uint_as_float(o[T][3].y));
#pragma unroll
      for (int i = 0; i < 4; i++) { const float d = __uint_as_float(o[T][i].x) - mu; m2 = fmaf(192.0f * d, d, m2); }
      const float rs_ = __builtin_amdgcn_rsqf(fmaf(m2, 1.0f / 768.0f, 1e-5f));
#pragma unroll
      for (int t = 0; t < 3; t++) { const f32x4 y = (z[t][T] - mu) * rs_; *(LAS u32x2*)(zi + (16 * T + col) * 104 + t * 32 + g * 8) = (u32x2){pk_bf16(y[0], y[1]), pk_bf16(y[2], y[3])}; }
    }
    WAIT_LGKM0();
#pragma unroll
    for (int t = 0; t < 3; t++) {
      LAS char* r = zi + (8 * g + (col >> 2)) * 104 + t * 32 + (lane & 3) * 8;
      const u32x2 r0 = tr_read(r), r1 = tr_read(r + 4 * 104);
      st16_dev((u32x4){r0.x, r0.y, r1.x, r1.y}, rz, l16, (unsigned)((sp * 48 + (sl - 4) * 12 + 3 * w + t) * 1024));
    }
  };
  bool have = false; int done = 0;
  bar();
  for (int s = gi; s < NSEG; s += 4, par ^= 1) {
    WS(0);
    if (!have) {
      if (sp >= 0) { st_load(); finish(); done = sp + 1; sp = -1; }
      if (done) { WAIT_VM(0); flag_set(rf, lane, FL_Z + (gi * 8 + sl) * 4 + w, done); done = 0; }
      flag_wait<false>(p, rf, lane, FL_H + (s % 3) * 32, 1, 32, s + 1, dead);
#pragma unroll
      for (int i = 0; i < 10; i++) h_dma(s, par, i);
      WAIT_VM(0);
    }
    bar();
    WS(1);
    const int sn = s + 4;
    int fh = (int)__builtin_amdgcn_raw_buffer_load_b32(rf, (unsigned)(lane & 31) * 128u, (unsigned)(FL_H + (sn % 3) * 32) * 128u, 16);
    f32x4 acc[3][2], s1[2], s2[2];
    LAS char* xb = lds + par * 40960 + l16;
    {
      LAS char* mb = xb + w * 10240; u32x4 b[10];
#pragma unroll
      for (int i = 0; i < 10; i++) b[i] = lds_r16(mb + i * 1024);
      SB();
#pragma unroll
      for (int i = 0; i < 10; i++) { if (i < 2) { mma_v0(s1[i], ones, b[i]); mma_v0(s2[i], b[i], b[i]); } else { mma_v(s1[i & 1], ones, b[i]); mma_v(s2[i & 1], b[i], b[i]); } }
      SB();
    }
    unsigned l_h = la + (par ^ 1) * 40960 + w * 10240, s_h = (unsigned)(sn * 40960 + w * 10240); PINS(l_h); PINS(s_h);
    gemm640<19>(wf, acc, xb,
                [&](auto KS) { tile_pre<KS.value - 4>(l_h); },
                [&](auto KS) {
                  constexpr int ks = KS.value;
                  if (ks == 1) asm volatile("" : "+v"(s1[0]), "+v"(s1[1]), "+v"(s2[0]), "+v"(s2[1]));
                  if (ks == 1 && g == (col >> 2)) {
#pragma unroll
                    for (int T = 0; T < 2; T++) { const int r = col & 3; *(LAS f32x2*)(st + par * 256 + ((w * 2 + T) * 16 + col) * 2) = (f32x2){s1[T][0], r == 0 ? s2[T][0] : r == 1 ? s2[T][1] : r == 2 ? s2[T][2] : s2[T][3]}; }
                  }
                  if (ks == 4) { PINV(fh); have = sn < NSEG && __builtin_amdgcn_ballot_w64(fh <= sn) == 0; }
                  if (ks >= 4 && ks < 14 && have) tile_dma<ks - 4, true>(sh, l16, s_h);
                  if (ks == 15 && sp >= 0) st_load();
                },
                [&]() { WAIT_VM(0); asm volatile("" : "+v"(o[0][0]), "+v"(o[0][1]), "+v"(o[0][2]), "+v"(o[0][3]), "+v"(o[1][0]), "+v"(o[1][1]), "+v"(o[1][2]), "+v"(o[1][3])); if (done) { flag_set(rf, lane, FL_Z + (gi * 8 + sl) * 4 + w, done); done = 0; } });
    WS(2);
    bar();
    WS(3);
    if (sp >= 0) { finish(); done = sp + 1; sp = -1; }
    WS(4);
#pragma unroll
    for (int T = 0; T < 2; T++) {
      f32x2 a = *(LAS f32x2*)(st + par * 256 + (T * 16 + col) * 2);
#pragma unroll
      for (int i = 1; i < 4; i++) a += *(LAS f32x2*)(st + par * 256 + ((i * 2 + T) * 16 + col) * 2);
      const float mean = a[0] * (1.0f / 640.0f), rstd = __builtin_amdgcn_rsqf(fmaxf(fmaf(-mean, mean, a[1] * (1.0f / 640.0f)), 0.f) + 1e-5f);
#pragma unroll
      for (int t = 0; t < 3; t++) z[t][T] = gelu_t(__builtin_elementwise_fma(cs[t], (f32x4){-mean, -mean, -mean, -mean}, acc[t][T]) * rstd);
    }
    WS(5);
    if (sl < 4) {
#pragma unroll
      for (int t = 0; t < 3; t++) { const int ft = sl * 12 + 3 * w + t;
        st16_dev(chunk_bf16(z[t][0], z[t][1]), rz, (unsigned)((g & 1) * 1024 + (g >> 1) * 256 + col * 16), (unsigned)(s * 49152 + (ft >> 1) * 2048 + (ft & 1) * 512)); }
      done = s + 1;
    } else {
#pragma unroll
      for (int T = 0; T < 2; T++) {
        const f32x4 sm = (z[0][T] + z[1][T]) + z[2][T];
        const float mw = xg_sum((sm[0] + sm[1]) + (sm[2] + sm[3])) * (1.0f / 48.0f);
        f32x4 q = {};
#pragma unroll
        for (int t = 0; t < 3; t++) { const f32x4 d = z[t][T] - mw; q = __builtin_elementwise_fma(d, d, q); }
        const float qw = xg_sum((q[0] + q[1]) + (q[2] + q[3]));
        if (g == 0) *(LAS f32x2*)(st + 512 + ((w * 2 + T) * 16 + col) * 2) = (f32x2){mw, qw};
      }
      bar();
      if (w == 0) {
        f32x2 a[4];
#pragma unroll
        for (int i = 0; i < 4; i++) a[i] = *(LAS f32x2*)(st + 512 + (i * 32 + (lane & 31)) * 2);
        const float mb = 0.25f * ((a[0][0] + a[1][0]) + (a[2][0] + a[3][0])); float qb = (a[0][1] + a[1][1]) + (a[2][1] + a[3][1]);
#pragma unroll
        for (int i = 0; i < 4; i++) { const float d = a[i][0] - mb; qb = fmaf(48.0f * d, d, qb); }
        __builtin_amdgcn_raw_buffer_store_b64((u32x2){san(__float_as_uint(mb)), san(__float_as_uint(qb))}, rs, lane < 32 ? (unsigned)lane * 8u : OOB, (unsigned)((s * 4 + sl - 4) * 256), 16);
      }
      sp = s;
    }
    WS(6); WS_FLUSH(4 + (sl >> 2), gi == 0 && (sl & 3) == 0);
    if (dead) return true;
  }
  if (sp >= 0) { st_load(); finish(); done = sp + 1; }
  WAIT_VM(0); flag_set(rf, lane, FL_Z + (gi * 8 + sl) * 4 + w, done);
  return dead;
}

DEV bool role_sp(const P& p, int tb, int fs) {
  const int lane = threadIdx.x & 63, w = __builtin_amdgcn_readfirstlane(threadIdx.x >> 6);
  const int g = lane >> 4, col = lane & 15; const unsigned l16 = lane * 16;
  const int wt = w >> 1, ft0 = 12 * fs + 6 * (w & 1), t0 = 128 * tb + 64 * wt;
  const rsrc_t rf = flag_rsrc(p), rz2 = mk_rsrc(p.ws + W_Z2, NSEG * 49152), rw = mk_rsrc(p.w_sp, SEQ * SEQ * 2);
  const rsrc_t rz1 = mk_rsrc(p.ws + W_Z1, NSEG * 49152), ru = mk_rsrc(p.ws + W_U, NSEG * 49152), rb = mk_rsrc(p.b_sp, SEQ * 2);
  const unsigned vw = (unsigned)(col * SEQ * 2 + g * 16), vz = (unsigned)((g & 1) * 1024 + (g >> 1) * 256 + col * 16);
  bool dead = false; int have[4] = {0, 0, 0, 0};
  u32x4 A[4][6], B[4][4], z[2][6];
  f32x4 acc[6][4];
#pragma unroll
  for (int k = 0; k < 4; k++) { const float b = bf_lo(__builtin_amdgcn_raw_buffer_load_b16(rb, (unsigned)(col * 2), (unsigned)((t0 + 16 * k) * 2), 0));
#pragma unroll
    for (int i = 0; i < 6; i++) { acc[i][k] = (f32x4){b, b, b, b}; asm volatile("" : "+a"(acc[i][k])); } }
  auto issueB = [&](int s, int q) {
#pragma unroll
    for (int i = 0; i < 4; i++) B[q][i] = ld16(rw, vw, (unsigned)((t0 + 16 * i) * SEQ * 2 + s * 64));
  };
  auto issueA = [&](int s, int q, auto NAP) {
    if (have[q] <= s) have[q] = flag_wait<NAP.value>(p, rf, lane, FL_Z + (q * 8 + 4 + fs) * 4 + 2 * (w & 1), 1, 2, s + 1, dead);
#pragma unroll
    for (int i = 0; i < 6; i++) A[q][i] = ld16_dev(rz2, l16, (unsigned)((s * 48 + ft0 + i) * 1024));
    SB();
  };
  auto prod = [&](int q) {
#pragma unroll
    for (int i = 0; i < 6; i++)
#pragma unroll
      for (int k = 0; k < 4; k++) acc[i][k] = mma(A[q][i], B[q][k], acc[i][k]);
    SB();
  };
  auto z1_load = [&](int sg) {
    const int s = 4 * tb + 2 * wt + sg;
    flag_wait<false>(p, rf, lane, FL_Z + ((s & 3) * 8 + fs) * 4 + 2 * (w & 1), 1, 2, s + 1, dead);
#pragma unroll
    for (int i = 0; i < 6; i++) z[sg][i] = ld16_dev(rz1, vz, (unsigned)(s * 49152 + ((ft0 + i) >> 1) * 2048 + (i & 1) * 512));
    SB();
  };
  issueB(0, 0); issueA(0, 0, std::true_type{}); issueB(1, 1); issueA(1, 1, std::true_type{});
  for (int s = 0; s < NSEG - 4; s += 4) {
#pragma unroll
    for (int q = 0; q < 4; q++) { issueB(s + q + 2, (q + 2) & 3); issueA(s + q + 2, (q + 2) & 3, std::true_type{}); prod(q); if (SSLEEP > 0) __builtin_amdgcn_s_sleep(SSLEEP); }
    if (dead) return true;
  }
  issueB(NSEG - 2, 2); issueB(NSEG - 1, 3);
  prod(0); prod(1);
  const bool lastb = 2 * tb + wt == 31;
  f32x4 zf[6][4];
  auto z1_unpack = [&](int sg) {
#pragma unroll
    for (int i = 0; i < 6; i++) { unsigned x0 = z[sg][i].x, x1 = z[sg][i].y, y0 = z[sg][i].z, y1 = z[sg][i].w; swap16(x0, y0); swap16(x1, y1);
      zf[i][2 * sg] = (f32x4){bf_lo(x0), bf_hi(x0), bf_lo(x1), bf_hi(x1)}; zf[i][2 * sg + 1] = (f32x4){bf_lo(y0), bf_hi(y0), bf_lo(y1), bf_hi(y1)}; PINV(zf[i][2 * sg]); PINV(zf[i][2 * sg + 1]); }
    SB();
  };
  z1_load(0); z1_unpack(0);
  if (!lastb) z1_load(1);
  issueA(NSEG - 2, 2, std::false_type{});
  prod(2);
  if (lastb) z1_load(1);
  z1_unpack(1);
  issueA(NSEG - 1, 3, std::false_type{});
  prod(3);
#pragma unroll
  for (int sg = 0; sg < 2; sg++) {
    const int s = 4 * tb + 2 * wt + sg;
#pragma unroll
    for (int i = 0; i < 6; i++) st16_dev(chunk_bf16(acc[i][2 * sg] * zf[i][2 * sg], acc[i][2 * sg + 1] * zf[i][2 * sg + 1]), ru, vz, (unsigned)(s * 49152 + ((ft0 + i) >> 1) * 2048 + (i & 1) * 512));
  }
  WAIT_VM(0);
  flag_set(rf, lane, FL_U + (tb * 2 + wt) * 8 + fs * 2 + (w & 1), 1);
  return dead;
}

DEV void role_fin(const P& p, int tb2, int cs, LAS char* lds) {
  const int lane = threadIdx.x & 63, w = __builtin_amdgcn_readfirstlane(threadIdx.x >> 6);
  const int g = lane >> 4, col = lane & 15; const unsigned l16 = lane * 16;
  bar();
  u32x4 wf[24][2];
  load_weights<2, 12, 3>(p.w_out, FH * DM * 2, DM * 2, lds + 98304 + w * 12288, lane, [&](int i) { return (unsigned)(i * 64 * DM + cs * 128 + w * 32) * 2u; },
                         [&](int i, int j, int t, u32x4 f) { wf[2 * i + j][t] = f; });
  const rsrc_t rf = flag_rsrc(p), rh = mk_rsrc(p.ws + W_H, NSEG * 40960), ry = mk_rsrc(p.out, SEQ * DM * 2);
  const u32x4 su = mk_srd(p.ws + W_U, NSEG * 49152);
  bool dead = false;
  u32x4 hv[2][2];
  flag_wait(p, rf, lane, FL_H + ((2 * tb2) % 3) * 32, 1, 32, 2 * tb2 + 1, dead); flag_wait(p, rf, lane, FL_H + ((2 * tb2 + 1) % 3) * 32, 1, 32, 2 * tb2 + 2, dead);
#pragma unroll
  for (int sg = 0; sg < 2; sg++)
#pragma unroll
    for (int t = 0; t < 2; t++) hv[sg][t] = ld16_dev(rh, (unsigned)((g & 1) * 1024 + (g >> 1) * 256 + col * 16), (unsigned)((2 * tb2 + sg) * 40960 + (4 * cs + w) * 2048 + t * 512));
  asm volatile("" : "+v"(hv[0][0]), "+v"(hv[0][1]), "+v"(hv[1][0]), "+v"(hv[1][1]));
  EV(4);
  flag_wait<false>(p, rf, lane, FL_U + tb2 * 8, 1, 8, 1, dead);
  EV(5);
  const unsigned la = (unsigned)(size_t)lds;
  auto u_dma = [&](int ks) { dma16_dev(su, l16, (unsigned)((2 * tb2 + (w >> 1)) * 49152 + ks * 2048 + (w & 1) * 1024), la + (ks * 4 + w) * 1024); };
#pragma unroll
  for (int ks = 0; ks < 8; ks++) u_dma(ks);
  f32x4 acc[2][4] = {};
  LAS char* ub = lds + l16; PINV(ub);
  static_for<0, 6>([&](auto I) {
    constexpr int c = I.value;
    wait_vm_c<(c < 5 ? 4 : 0)>();
    bar();
    u32x4 b[2][4];
#pragma unroll
    for (int k = 0; k < 4; k++) b[0][k] = lds_r16(ub + (16 * c + k) * 1024);
#pragma unroll
    for (int i = 0; i < 4; i++) {
      const int ks = 4 * c + i;
      if (i < 3) {
#pragma unroll
        for (int k = 0; k < 4; k++) b[(i + 1) & 1][k] = lds_r16(ub + ((ks + 1) * 4 + k) * 1024);
      }
      SB();
#pragma unroll
      for (int k = 0; k < 4; k++) { acc[0][k] = mma(wf[ks][0], b[i & 1][k], acc[0][k]); acc[1][k] = mma(wf[ks][1], b[i & 1][k], acc[1][k]); if (k == 0 && c < 4) { SB(); u_dma(ks + 8); SB(); } }
      SB();
    }
  });
#pragma unroll
  for (int sg = 0; sg < 2; sg++)
#pragma unroll
    for (int t = 0; t < 2; t++) {
      f32x4 a = acc[t][2 * sg], b = acc[t][2 * sg + 1]; chunk_f32(a, b); const u32x4 x = hv[sg][t];
      const u32x4 o = {pk_bf16(a[0] + bf_lo(x.x), a[1] + bf_hi(x.x)), pk_bf16(a[2] + bf_lo(x.y), a[3] + bf_hi(x.y)), pk_bf16(b[0] + bf_lo(x.z), b[1] + bf_hi(x.z)), pk_bf16(b[2] + bf_lo(x.w), b[3] + bf_hi(x.w))};
      __builtin_amdgcn_raw_buffer_store_b128(o, ry, (unsigned)(((16 * (g & 1) + col) * DM + 8 * (g >> 1)) * 2), (unsigned)(((2 * tb2 + sg) * 32 * DM + cs * 128 + w * 32 + t * 16) * 2), 0);
      st_guard(o);
    }
}
__global__ void __launch_bounds__(256) prep_kernel(P p) {
  const int lane = threadIdx.x & 63, w = __builtin_amdgcn_readfirstlane(threadIdx.x >> 6), b = blockIdx.x;
  if (b >= 256) {
    const int vt = (b - 256) * 4 + w;
    const rsrc_t rp = mk_rsrc(p.ws + W_RING + (124 + (vt >> 4)) * CH, CH);
    const unsigned o = (unsigned)(vt & 15) * 512u;
    __builtin_amdgcn_raw_buffer_store_b32(0u, rp, (unsigned)((lane >> 5) * 8192 + (lane & 31) * 4), o, 16);
    __builtin_amdgcn_raw_buffer_store_b32(0u, rp, (unsigned)((lane >> 5) * 8192 + (lane & 31) * 4), o + 256, 16);
    WAIT_VM(0);
    int t[2] = {1 << 20, 1 << 20};
#pragma unroll
    for (int i = 0; i < 4; i++) {
      unsigned long long c0 = __builtin_amdgcn_s_memtime(); asm volatile("s_waitcnt lgkmcnt(0)" : "+s"(c0) :: "memory");
      unsigned v = __builtin_amdgcn_raw_buffer_load_b32(rp, 0, o + (unsigned)(((i ^ w) & 1) * 8192 + (i >> 1) * 256), 16);
      asm volatile("s_waitcnt vmcnt(0)" : "+v"(v) :: "memory");
      unsigned long long c1 = __builtin_amdgcn_s_memtime(); asm volatile("s_waitcnt lgkmcnt(0)" : "+s"(c1) :: "memory");
      const int d = (int)(c1 - c0);
      if (((i ^ w) & 1) == 0) t[0] = min(t[0], d); else t[1] = min(t[1], d);
    }
    if (lane == 0) ((unsigned*)(p.ws + W_FLAG))[FL_PAR * 32 + vt] = PARFLIP ^ (t[0] > t[1] ? 1u : 0u) ^ ((b >> 2) & 1);
    return;
  }
  const int row = b * 8 + w * 2 + (lane >> 5), l = lane & 31;
  const u32x4* xr = (const u32x4*)((const char*)p.x + (size_t)row * DM * 2);
  u32x4 v[3]; float f[24]; float sm = 0.f;
#pragma unroll
  for (int i = 0; i < 3; i++) { const int c = l + 32 * i; v[i] = c < 80 ? xr[c] : (u32x4){0, 0, 0, 0}; }
#pragma unroll
  for (int i = 0; i < 12; i++) { f[2 * i] = bf_lo(v[i >> 2][i & 3]); f[2 * i + 1] = bf_hi(v[i >> 2][i & 3]); sm += f[2 * i] + f[2 * i + 1]; }
#pragma unroll
  for (int o = 1; o < 32; o <<= 1) sm += __shfl_xor(sm, o);
  const float mean = sm * (1.0f / DM); float q = 0.f;
#pragma unroll
  for (int i = 0; i < 24; i++) { const float d = f[i] - mean; if (i < 16 || l < 16) q = fmaf(d, d, q); }
#pragma unroll
  for (int o = 1; o < 32; o <<= 1) q += __shfl_xor(q, o);
  const float rstd = __builtin_amdgcn_rsqf(fmaf(q, 1.0f / DM, 1e-5f));
#pragma unroll
  for (int i = 0; i < 3; i++) { const int c = l + 32 * i;
    if (c < 80) { u32x4 o;
#pragma unroll
      for (int k = 0; k < 4; k++) o[k] = pk_bf16((f[8 * i + 2 * k] - mean) * rstd, (f[8 * i + 2 * k + 1] - mean) * rstd);
      *(u32x4*)(p.ws + W_XN + (size_t)(row >> 5) * 40960 + ((c >> 2) * 2 + ((row >> 4) & 1)) * 1024 + ((c & 3) * 16 + (row & 15)) * 16) = o; } }
  const int t = b * 256 + threadIdx.x;
#pragma unroll
  for (int i = 0; i < 2; i++) {
    const unsigned o = (unsigned)(t + i * 65536) * 16u, c = o >> 14, in = o & 8191u;
    u32x4 z = {SENT, SENT, SENT, SENT};
    if (c == 0 || (c == 1 && in < 2048)) { const int ks = c * 8 + (in >> 10), r = (in >> 4) & 15;
      z = *(const u32x4*)((const char*)p.mem0 + ((2 * (r >> 2) + (r & 1)) * DM + 64 * ks + 16 * ((in >> 8) & 3) + 8 * ((r >> 1) & 1)) * 2);
      z.x = san(z.x); z.y = san(z.y); z.z = san(z.z); z.w = san(z.w); }
    if (c < 98) *(u32x4*)(p.ws + W_RING + o) = z;
  }
  if (t < 10240) { if ((t >> 4) != FL_PAR / 2) *(u32x4*)(p.ws + W_FLAG + t * 16) = (u32x4){0, 0, 0, 0}; }
  else if (t < 14336) *(u32x4*)(p.ws + W_STAT + (t - 10240) * 16) = (u32x4){SENT, SENT, SENT, SENT};
}

constexpr int MEGA_LDS = 147456;
__global__ void __launch_bounds__(256, 1) mega_kernel(P p) {
  extern __shared__ __attribute__((aligned(16))) char smem[];
  LAS char* lds = (LAS char*)smem;
  const int x = blockIdx.x & 7, r = blockIdx.x >> 3;
  int fin;
  EV(0);
  if (x < 4) {
    bool dead;
    if (WARM && r >= 16) { const int h = 4 * (r & 7) + x; if (r < 24) warm_rows(p.Wk, h * DH * 2, NH * DH * 2, 0, DM, 24); else warm_rows(p.Wv, h * DH * 2, NH * DH * 2, 0, DM, 24); }
#define PT 0
    if (PT) {
      const rsrc_t rw = mk_rsrc(p.ws, (unsigned)W_DBG);
      unsigned v = 0;
      for (unsigned o = threadIdx.x * (unsigned)PT; o < (unsigned)W_DBG; o += 256u * PT) v += __builtin_amdgcn_raw_buffer_load_b32(rw, o, 0, 2);
      asm volatile("" :: "v"(v));
    }
    if (r < 8) dead = role_qk(p, 4 * r + x, lds);
    else if (r < 16) dead = role_vo(p, 4 * (r - 8) + x, lds);
    else if (r < 26) dead = role_red(p, 10 * x + r - 16, lds);
    else dead = role_out(p, x, (r - 26) >> 1, (r - 26) & 1, lds);
    if (dead) return;
    EV(2);
    fin = x * 32 + r;
  } else {
    const int h = 8 * (x - 4) + (r & 7), kind = r >> 3;
    if (kind == 0) { if (!role_proj<0>(p, h, lds)) { EV(2); role_in(p, h >> 3, h & 7, lds); EV(3); } return; }
    if (kind == 3) { if (role_proj<2>(p, h, lds)) return; EV(2); fin = 128 + h; }
    else {
      if (kind == 1 ? role_proj<1>(p, h, lds) : role_o(p, h, lds)) return;
      const int i = r - 8; EV(2);
      role_sp(p, 4 * (x - 4) + (i >> 2), i & 3); EV(3);
      return;
    }
  }
  role_fin(p, fin / 5, fin % 5, lds);
  EV(3);
}
#define P_DQ 207
#define P_DV 3100
#define P_DR 3350
#define P_DX 50
static void launch_all(void* const* d_in, void* d_out, void* d_ws, hipStream_t stream, int dq, int dv, int dr, int dx) {
  P p;
  p.x = d_in[0]; p.mem0 = d_in[1]; p.Wq = d_in[2]; p.Wk = d_in[3]; p.Wv = d_in[4]; p.Wo = d_in[5];
  p.w_in = d_in[8]; p.w_sp = d_in[9]; p.b_sp = d_in[10]; p.w_out = d_in[11];
  p.out = d_out;
  p.ws = (char*)d_ws + ((RING_BYTES - ((size_t)d_ws & (RING_BYTES - 1))) & (RING_BYTES - 1));
  p.dq = dq; p.dv = dv; p.dr = dr; p.dx = dx;
  (void)hipFuncSetAttribute((const void*)mega_kernel, hipFuncAttributeMaxDynamicSharedMemorySize, MEGA_LDS);
  prep_kernel<<<272, 256, 0, stream>>>(p);
  mega_kernel<<<256, 256, MEGA_LDS, stream>>>(p);
}
extern "C" void kernel_launch(void* const* d_in, const int* in_sizes, int n_in, void* d_out, int out_size, void* d_ws, size_t ws_size, hipStream_t stream) {
  launch_all(d_in, d_out, d_ws, stream, P_DQ, P_DV, P_DR, P_DX);
}
```
